```python
import torch

def _softmax(x, axis=-1):
    return torch.softmax(x, dim=axis)
def _gelu(x):
    return torch.nn.functional.gelu(x, approximate="tanh")
def _silu(x):
    return torch.nn.functional.silu(x)
def _sigmoid(x):
    return torch.sigmoid(x)
def _layernorm(x, axis=-1, eps=1e-5):
    m = x.mean(dim=axis, keepdim=True)
    v = x.var(dim=axis, keepdim=True, unbiased=False)
    return (x - m) / torch.sqrt(v + eps)
def _scatter_add(tgt, idx, src, axis=0):
    return tgt.clone().index_add_(axis, idx, src)
def _segment_sum(src, seg, n):
    out = torch.zeros((n,) + src.shape[1:], dtype=src.dtype)
    return out.index_add_(0, seg, src)
def _pad(x, pw):
    flat = tuple(v for pair in reversed(pw) for v in pair)
    return torch.nn.functional.pad(x, flat)

kernel_name = 'gated_mix__grouped_kv_mqa__sliding_local_dilated__prompt'

def setup_inputs():

    _w = (lambda a: (lambda t: t.to(torch.bfloat16) if t.is_floating_point() else t)(torch.as_tensor(a)))
    def _A_setup():
        B, S, D, H = 1, 2048, 640, 32
        Dh = D // H
        return {
            "x": _w((lambda *s: torch.randn(*s, dtype=torch.bfloat16) * 0.05)(B, S, D)),
            "wq": _w((lambda *s: torch.randn(*s, dtype=torch.bfloat16) * 1.6)(D, H * Dh)),
            "wk": _w((lambda *s: torch.randn(*s, dtype=torch.bfloat16) * 1.6)(D, Dh)),
            "wv": _w((lambda *s: torch.randn(*s, dtype=torch.bfloat16) * 0.2)(D, Dh)),
            "wo": _w((lambda *s: torch.randn(*s, dtype=torch.bfloat16) * 0.2)(H * Dh, D)),
        }
    _A_in = _A_setup()

    def _B_setup():
        B, S, D = 1, 2048, 640
        return {
            "x": _w((lambda *s: torch.randn(*s, dtype=torch.bfloat16) * 0.05)(B, S, D)),
            "wqkv": _w((lambda *s: torch.randn(*s, dtype=torch.bfloat16) * 0.8)(D, 3 * D)),
            "wo": _w((lambda *s: torch.randn(*s, dtype=torch.bfloat16) * 0.05)(D, D)),
        }
    _B_in = _B_setup()

    return {
        "A_x": _A_in["x"],
        "A_wq": _A_in["wq"],
        "A_wk": _A_in["wk"],
        "A_wv": _A_in["wv"],
        "A_wo": _A_in["wo"],
        "B_x": _B_in["x"],
        "B_wqkv": _B_in["wqkv"],
        "B_wo": _B_in["wo"],
        "gate_w": _w((lambda *s: torch.randn(*s, dtype=torch.bfloat16) * 0.05)(640) * 16.0),
    }

def reference(A_x, A_wq, A_wk, A_wv, A_wo, B_x, B_wqkv, B_wo, gate_w):
    def _A_ref(x, wq, wk, wv, wo):
        B, S, D = x.shape
        H = 32
        Dh = D // H
        q = (lambda x, *d: x.permute(*d))((x @ wq).reshape(B, S, H, Dh), 0, 2, 1, 3)
        k = x @ wk
        v = x @ wv
        scores = torch.einsum("bhqd,bkd->bhqk", q, k) * (Dh ** -0.5)
        p = _softmax(scores, axis=-1)
        o = torch.einsum("bhqk,bkd->bhqd", p, v)
        return (lambda x, *d: x.permute(*d))(o, 0, 2, 1, 3).reshape(B, S, H * Dh) @ wo

    def _B_ref(x, wqkv, wo):
        B, S, D = x.shape
        H = 32; Dh = D // H
        W, dil = 8, 2
        qkv = (lambda x, *d: x.permute(*d))((x @ wqkv).reshape(B, S, 3, H, Dh), 2, 0, 3, 1, 4)
        q, k, v = qkv[0], qkv[1], qkv[2]
        scale = Dh ** -0.5
        pos = torch.arange(S)
        off = torch.arange(W) * dil
        idx = pos[:, None] - off[None, :]
        mask = idx >= 0
        idx_c = torch.where(mask, idx, 0).reshape(-1)
        kg = k[:, :, idx_c, :].reshape(B, H, S, W, Dh)
        vg = v[:, :, idx_c, :].reshape(B, H, S, W, Dh)
        logits = torch.einsum("bhsd,bhswd->bhsw", q, kg) * scale
        logits = torch.where(mask[None, None, :, :], logits, -1e9)
        attn = _softmax(logits, axis=-1)
        o = torch.einsum("bhsw,bhswd->bhsd", attn, vg)
        return (lambda x, *d: x.permute(*d))(o, 0, 2, 1, 3).reshape(B, S, D) @ wo

    x = A_x
    g = _sigmoid((x * gate_w).sum(-1, keepdims=True))
    a = _A_ref(x, A_wq, A_wk, A_wv, A_wo)
    b = _B_ref(x, B_wqkv, B_wo)
    return g * a + (1 - g) * b
```

```cpp
#include <hip/hip_runtime.h>
#include <hip/hip_bf16.h>
typedef __hip_bfloat16 bf16;
static __device__ __forceinline__ float b2f(bf16 v) { return __bfloat162float(v); }
__global__ void gemm_naive(const bf16* A, const bf16* W, float* C, int M, int N, int K, float scale) {
  int n = blockIdx.x * 64 + threadIdx.x, m = blockIdx.y;
  if (n >= N) return;
  float acc = 0.f;
  for (int k = 0; k < K; ++k) acc += b2f(A[m * K + k]) * b2f(W[k * N + n]);
  C[m * N + n] = b2f(__float2bfloat16(acc * scale));
}
__global__ void attnA(const float* q, const float* k, const float* v, bf16* o) {
  int idx = blockIdx.x * 64 + threadIdx.x;
  int s = idx / 32, h = idx % 32;
  float qq[20]; for (int d = 0; d < 20; ++d) qq[d] = q[s * 640 + h * 20 + d];
  float m = -1e30f, l = 0.f, acc[20]; for (int d = 0; d < 20; ++d) acc[d] = 0.f;
  for (int j = 0; j < 2048; ++j) {
    float sc = 0.f; for (int d = 0; d < 20; ++d) sc += qq[d] * k[j * 20 + d];
    float mn = fmaxf(m, sc), a = exp2f(m - mn), p = exp2f(sc - mn);
    l = l * a + p; for (int d = 0; d < 20; ++d) acc[d] = acc[d] * a + p * v[j * 20 + d];
    m = mn;
  }
  for (int d = 0; d < 20; ++d) o[s * 640 + h * 20 + d] = __float2bfloat16(acc[d] / l);
}
__global__ void attnB(const float* qkv, bf16* o) {
  int idx = blockIdx.x * 64 + threadIdx.x; int s = idx / 32, h = idx % 32;
  float lg[8], m = -1e30f;
  for (int w = 0; w < 8; ++w) { int j = s - 2 * w; float sc = -1e9f; if (j >= 0) { sc = 0.f; for (int d = 0; d < 20; ++d) sc += qkv[s * 1920 + h * 20 + d] * qkv[j * 1920 + 640 + h * 20 + d]; sc *= 0.22360679775f; } lg[w] = sc; m = fmaxf(m, sc); }
  float l = 0.f; for (int w = 0; w < 8; ++w) { lg[w] = expf(lg[w] - m); l += lg[w]; }
  for (int d = 0; d < 20; ++d) { float a = 0.f; for (int w = 0; w < 8; ++w) { int j = s - 2 * w; if (j < 0) j = 0; a += lg[w] * qkv[j * 1920 + 1280 + h * 20 + d]; } o[s * 640 + h * 20 + d] = __float2bfloat16(a / l); }
}
__global__ void fin(const bf16* x, const bf16* gw, const float* a, const float* b, bf16* out) {
  int s = blockIdx.x; __shared__ float red[64];
  float t = 0.f; for (int d = threadIdx.x; d < 640; d += 64) t += b2f(x[s * 640 + d]) * b2f(gw[d]);
  red[threadIdx.x] = t; __syncthreads();
  t = 0.f; for (int i = 0; i < 64; ++i) t += red[i];
  float g = 1.f / (1.f + expf(-t));
  for (int d = threadIdx.x; d < 640; d += 64) out[s * 640 + d] = __float2bfloat16(g * a[s * 640 + d] + (1.f - g) * b[s * 640 + d]);
}
extern "C" void kernel_launch(void* const* d_in, const int* in_sizes, int n_in, void* d_out, int out_size, void* d_ws, size_t ws_size, hipStream_t stream) {
  const bf16* x = (const bf16*)d_in[0]; const bf16* wq = (const bf16*)d_in[1]; const bf16* wk = (const bf16*)d_in[2]; const bf16* wv = (const bf16*)d_in[3];
  const bf16* woA = (const bf16*)d_in[4]; const bf16* wqkv = (const bf16*)d_in[6]; const bf16* woB = (const bf16*)d_in[7]; const bf16* gw = (const bf16*)d_in[8];
  float* q = (float*)d_ws; float* k = q + 2048 * 640; float* v = k + 2048 * 20; float* qkv = v + 2048 * 20; float* a = qkv + 2048 * 1920; float* b = a + 2048 * 640;
  bf16* oA = (bf16*)(b + 2048 * 640); bf16* oB = oA + 2048 * 640;
  gemm_naive<<<dim3(10, 2048), 64, 0, stream>>>(x, wq, q, 2048, 640, 640, 0.22360679775f * 1.44269504089f);
  gemm_naive<<<dim3(1, 2048), 64, 0, stream>>>(x, wk, k, 2048, 20, 640, 1.f);
  gemm_naive<<<dim3(1, 2048), 64, 0, stream>>>(x, wv, v, 2048, 20, 640, 1.f);
  gemm_naive<<<dim3(30, 2048), 64, 0, stream>>>(x, wqkv, qkv, 2048, 1920, 640, 1.f);
  attnA<<<1024, 64, 0, stream>>>(q, k, v, oA);
  attnB<<<1024, 64, 0, stream>>>(qkv, oB);
  gemm_naive<<<dim3(10, 2048), 64, 0, stream>>>(oA, woA, a, 2048, 640, 640, 1.f);
  gemm_naive<<<dim3(10, 2048), 64, 0, stream>>>(oB, woB, b, 2048, 640, 640, 1.f);
  fin<<<2048, 64, 0, stream>>>(x, gw, a, b, (bf16*)d_out);
}
```

```cpp
#include <hip/hip_runtime.h>
#include <hip/hip_bf16.h>
#include <stdint.h>

typedef __attribute__((ext_vector_type(8))) __bf16 bf16x8;
typedef __attribute__((ext_vector_type(2))) __bf16 bf16x2;
typedef __attribute__((ext_vector_type(4))) short s16x4;
typedef __attribute__((ext_vector_type(16))) float f32x16;
typedef __attribute__((ext_vector_type(4))) float f32x4;
typedef __attribute__((ext_vector_type(2))) float f32x2;
typedef __attribute__((ext_vector_type(4))) unsigned u32x4;
typedef __attribute__((ext_vector_type(2))) unsigned u32x2;
#define LAS __attribute__((address_space(3)))
#define GAS __attribute__((address_space(1)))
typedef LAS char* lptr;
typedef GAS unsigned gu32;
#define RLX_AGENT __ATOMIC_RELAXED, __HIP_MEMORY_SCOPE_AGENT
#define RLX_WG __ATOMIC_RELAXED, __HIP_MEMORY_SCOPE_WORKGROUP
#define DEVI __device__ __forceinline__

constexpr int TILE_B = 3584;
constexpr unsigned WS_EPOCH = 0;
constexpr unsigned WS_GRAN = 256;
constexpr unsigned WS_HF = WS_GRAN + 8192;
constexpr unsigned WS_RF = WS_HF + 256 * 128;
constexpr unsigned WS_PF = WS_RF + 32 * 128;
constexpr unsigned WS_PG = WS_PF + 16 * 128;
constexpr unsigned WS_KV = WS_PG + 16 * 128;
constexpr unsigned WS_HALO = WS_KV + 64 * TILE_B;
constexpr unsigned WS_OB = WS_HALO + 256 * 5120;
constexpr unsigned WS_OA = WS_OB + 256 * 10240;
constexpr unsigned WS_PQ = WS_OA + 256 * 10240;
constexpr unsigned WS_PA = WS_PQ + 8 * 327680;
constexpr unsigned WS_PB = WS_PA + 8 * 102400;
constexpr unsigned WS_END = WS_PB + 8 * 102400;
constexpr int AUX_SC1 = 16;
constexpr unsigned SPIN_CAP = 1u << 18, DATA_CAP = 1u << 14;
constexpr float QSCALE = 0.22360679774997896f * 1.4426950408889634f;

#define ATT_ASM \
  "s_mov_b32 s40, %15\n" \
  "s_mov_b32 s41, %16\n" \
  "global_load_dwordx4 v[200:203], %12, s[40:41] offset:0 sc1\n" \
  "global_load_dwordx2 v[248:249], %13, s[40:41] offset:0 sc1\n" \
  "global_load_dwordx4 v[204:207], %14, s[40:41] offset:0 sc1\n" \
  "global_load_dwordx4 v[208:211], %14, s[40:41] offset:1024 sc1\n" \
  "global_load_dwordx4 v[212:215], %12, s[40:41] offset:3584 sc1\n" \
  "global_load_dwordx2 v[250:251], %13, s[40:41] offset:3584 sc1\n" \
  "s_add_u32 s40, s40, 3584\n" \
  "s_addc_u32 s41, s41, 0\n" \
  "global_load_dwordx4 v[216:219], %14, s[40:41] offset:0 sc1\n" \
  "global_load_dwordx4 v[220:223], %14, s[40:41] offset:1024 sc1\n" \
  "global_load_dwordx4 v[224:227], %12, s[40:41] offset:3584 sc1\n" \
  "global_load_dwordx2 v[252:253], %13, s[40:41] offset:3584 sc1\n" \
  "s_add_u32 s40, s40, 3584\n" \
  "s_addc_u32 s41, s41, 0\n" \
  "global_load_dwordx4 v[228:231], %14, s[40:41] offset:0 sc1\n" \
  "global_load_dwordx4 v[232:235], %14, s[40:41] offset:1024 sc1\n" \
  "global_load_dwordx4 v[236:239], %12, s[40:41] offset:3584 sc1\n" \
  "global_load_dwordx2 v[254:255], %13, s[40:41] offset:3584 sc1\n" \
  "v_mov_b32 v188, 0\n" \
  "v_mov_b32 v196, 0\n" \
  "v_mov_b32 v244, 0\n" \
  "v_mov_b32 v184, 0\n" \
  "v_mov_b32 v240, 0\n" \
  "v_mov_b32 v189, 0\n" \
  "v_mov_b32 v197, 0\n" \
  "v_mov_b32 v245, 0\n" \
  "v_mov_b32 v185, 0\n" \
  "v_mov_b32 v241, 0\n" \
  "v_mov_b32 v190, 0\n" \
  "v_mov_b32 v198, 0\n" \
  "v_mov_b32 v246, 0\n" \
  "v_mov_b32 v186, 0\n" \
  "v_mov_b32 v242, 0\n" \
  "v_mov_b32 v191, 0\n" \
  "v_mov_b32 v199, 0\n" \
  "v_mov_b32 v247, 0\n" \
  "v_mov_b32 v187, 0\n" \
  "v_mov_b32 v243, 0\n" \
  "s_waitcnt vmcnt(12)\n" \
  "v_mfma_f32_32x32x16_bf16 v[152:167], v[200:203], %4, 0\n" \
  "v_mfma_f32_32x32x8bf16_1k v[152:167], v[248:249], %8, v[152:167]\n" \
  "s_nop 7\n" \
  "s_nop 7\n" \
  "s_mov_b32 s42, 4\n" \
  "s_setprio 3\n" \
  "1:\n" \
  "v_mfma_f32_32x32x16_bf16 %3, v[240:243], v[184:187], %3\n" \
  "v_exp_f32 v192, v152\n" \
  "v_exp_f32 v193, v153\n" \
  "v_exp_f32 v194, v154\n" \
  "v_mfma_f32_32x32x16_bf16 v[168:183], v[200:203], %5, 0\n" \
  "v_exp_f32 v195, v155\n" \
  "v_exp_f32 v196, v156\n" \
  "v_exp_f32 v197, v157\n" \
  "v_mfma_f32_32x32x16_bf16 %3, v[244:247], v[188:191], %3\n" \
  "v_exp_f32 v198, v158\n" \
  "v_cvt_pk_bf16_f32 v184, v192, v193\n" \
  "v_cvt_pk_bf16_f32 v185, v194, v195\n" \
  "v_exp_f32 v199, v159\n" \
  "v_mfma_f32_32x32x8bf16_1k v[168:183], v[248:249], %9, v[168:183]\n" \
  "v_cvt_pk_bf16_f32 v186, v196, v197\n" \
  "s_setprio 0\n" \
  "v_exp_f32 v192, v160\n" \
  "v_exp_f32 v193, v161\n" \
  "v_cvt_pk_bf16_f32 v187, v198, v199\n" \
  "v_exp_f32 v194, v162\n" \
  "v_exp_f32 v195, v163\n" \
  "v_exp_f32 v196, v164\n" \
  "v_cvt_pk_bf16_f32 v188, v192, v193\n" \
  "v_exp_f32 v197, v165\n" \
  "s_setprio 3\n" \
  "v_cvt_pk_bf16_f32 v189, v194, v195\n" \
  "v_exp_f32 v198, v166\n" \
  "v_exp_f32 v199, v167\n" \
  "v_cvt_pk_bf16_f32 v190, v196, v197\n" \
  "v_cvt_pk_bf16_f32 v191, v198, v199\n" \
  "s_waitcnt vmcnt(10)\n" \
  "v_mfma_f32_32x32x16_bf16 %0, v[204:207], v[184:187], %0\n" \
  "s_add_u32 s40, s40, 3584\n" \
  "s_addc_u32 s41, s41, 0\n" \
  "global_load_dwordx4 v[240:243], %14, s[40:41] offset:0 sc1\n" \
  "v_exp_f32 v192, v168\n" \
  "v_exp_f32 v193, v169\n" \
  "v_exp_f32 v194, v170\n" \
  "v_mfma_f32_32x32x16_bf16 v[152:167], v[200:203], %6, 0\n" \
  "global_load_dwordx4 v[244:247], %14, s[40:41] offset:1024 sc1\n" \
  "v_exp_f32 v195, v171\n" \
  "v_exp_f32 v196, v172\n" \
  "v_exp_f32 v197, v173\n" \
  "v_mfma_f32_32x32x16_bf16 %0, v[208:211], v[188:191], %0\n" \
  "v_exp_f32 v198, v174\n" \
  "v_cvt_pk_bf16_f32 v184, v192, v193\n" \
  "v_cvt_pk_bf16_f32 v185, v194, v195\n" \
  "v_exp_f32 v199, v175\n" \
  "v_mfma_f32_32x32x8bf16_1k v[152:167], v[248:249], %10, v[152:167]\n" \
  "v_cvt_pk_bf16_f32 v186, v196, v197\n" \
  "s_setprio 0\n" \
  "v_exp_f32 v192, v176\n" \
  "v_exp_f32 v193, v177\n" \
  "v_cvt_pk_bf16_f32 v187, v198, v199\n" \
  "v_exp_f32 v194, v178\n" \
  "v_exp_f32 v195, v179\n" \
  "v_exp_f32 v196, v180\n" \
  "v_cvt_pk_bf16_f32 v188, v192, v193\n" \
  "v_exp_f32 v197, v181\n" \
  "s_setprio 3\n" \
  "v_cvt_pk_bf16_f32 v189, v194, v195\n" \
  "v_exp_f32 v198, v182\n" \
  "v_exp_f32 v199, v183\n" \
  "v_cvt_pk_bf16_f32 v190, v196, v197\n" \
  "v_cvt_pk_bf16_f32 v191, v198, v199\n" \
  "v_mfma_f32_32x32x16_bf16 %1, v[204:207], v[184:187], %1\n" \
  "v_exp_f32 v192, v152\n" \
  "v_exp_f32 v193, v153\n" \
  "v_exp_f32 v194, v154\n" \
  "v_mfma_f32_32x32x16_bf16 v[168:183], v[200:203], %7, 0\n" \
  "v_exp_f32 v195, v155\n" \
  "v_exp_f32 v196, v156\n" \
  "v_exp_f32 v197, v157\n" \
  "v_mfma_f32_32x32x16_bf16 %1, v[208:211], v[188:191], %1\n" \
  "v_exp_f32 v198, v158\n" \
  "v_cvt_pk_bf16_f32 v184, v192, v193\n" \
  "v_cvt_pk_bf16_f32 v185, v194, v195\n" \
  "v_exp_f32 v199, v159\n" \
  "v_mfma_f32_32x32x8bf16_1k v[168:183], v[248:249], %11, v[168:183]\n" \
  "global_load_dwordx4 v[200:203], %12, s[40:41] offset:3584 sc1\n" \
  "v_cvt_pk_bf16_f32 v186, v196, v197\n" \
  "s_setprio 0\n" \
  "v_exp_f32 v192, v160\n" \
  "v_exp_f32 v193, v161\n" \
  "v_cvt_pk_bf16_f32 v187, v198, v199\n" \
  "v_exp_f32 v194, v162\n" \
  "v_exp_f32 v195, v163\n" \
  "v_exp_f32 v196, v164\n" \
  "v_cvt_pk_bf16_f32 v188, v192, v193\n" \
  "v_exp_f32 v197, v165\n" \
  "s_setprio 3\n" \
  "v_cvt_pk_bf16_f32 v189, v194, v195\n" \
  "v_exp_f32 v198, v166\n" \
  "v_exp_f32 v199, v167\n" \
  "v_cvt_pk_bf16_f32 v190, v196, v197\n" \
  "v_cvt_pk_bf16_f32 v191, v198, v199\n" \
  "global_load_dwordx2 v[248:249], %13, s[40:41] offset:3584 sc1\n" \
  "v_mfma_f32_32x32x16_bf16 %2, v[204:207], v[184:187], %2\n" \
  "v_exp_f32 v192, v168\n" \
  "v_exp_f32 v193, v169\n" \
  "v_exp_f32 v194, v170\n" \
  "s_waitcnt vmcnt(12)\n" \
  "v_mfma_f32_32x32x16_bf16 v[152:167], v[212:215], %4, 0\n" \
  "v_exp_f32 v195, v171\n" \
  "v_exp_f32 v196, v172\n" \
  "v_exp_f32 v197, v173\n" \
  "v_mfma_f32_32x32x16_bf16 %2, v[208:211], v[188:191], %2\n" \
  "v_exp_f32 v198, v174\n" \
  "v_cvt_pk_bf16_f32 v184, v192, v193\n" \
  "v_cvt_pk_bf16_f32 v185, v194, v195\n" \
  "v_exp_f32 v199, v175\n" \
  "v_mfma_f32_32x32x8bf16_1k v[152:167], v[250:251], %8, v[152:167]\n" \
  "v_cvt_pk_bf16_f32 v186, v196, v197\n" \
  "s_setprio 0\n" \
  "v_exp_f32 v192, v176\n" \
  "v_exp_f32 v193, v177\n" \
  "v_cvt_pk_bf16_f32 v187, v198, v199\n" \
  "v_exp_f32 v194, v178\n" \
  "v_exp_f32 v195, v179\n" \
  "v_exp_f32 v196, v180\n" \
  "v_cvt_pk_bf16_f32 v188, v192, v193\n" \
  "v_exp_f32 v197, v181\n" \
  "s_setprio 3\n" \
  "v_cvt_pk_bf16_f32 v189, v194, v195\n" \
  "v_exp_f32 v198, v182\n" \
  "v_exp_f32 v199, v183\n" \
  "v_cvt_pk_bf16_f32 v190, v196, v197\n" \
  "v_cvt_pk_bf16_f32 v191, v198, v199\n" \
  "v_mfma_f32_32x32x16_bf16 %3, v[204:207], v[184:187], %3\n" \
  "v_exp_f32 v192, v152\n" \
  "v_exp_f32 v193, v153\n" \
  "v_exp_f32 v194, v154\n" \
  "v_mfma_f32_32x32x16_bf16 v[168:183], v[212:215], %5, 0\n" \
  "v_exp_f32 v195, v155\n" \
  "v_exp_f32 v196, v156\n" \
  "v_exp_f32 v197, v157\n" \
  "v_mfma_f32_32x32x16_bf16 %3, v[208:211], v[188:191], %3\n" \
  "v_exp_f32 v198, v158\n" \
  "v_cvt_pk_bf16_f32 v184, v192, v193\n" \
  "v_cvt_pk_bf16_f32 v185, v194, v195\n" \
  "v_exp_f32 v199, v159\n" \
  "v_mfma_f32_32x32x8bf16_1k v[168:183], v[250:251], %9, v[168:183]\n" \
  "v_cvt_pk_bf16_f32 v186, v196, v197\n" \
  "s_setprio 0\n" \
  "v_exp_f32 v192, v160\n" \
  "v_exp_f32 v193, v161\n" \
  "v_cvt_pk_bf16_f32 v187, v198, v199\n" \
  "v_exp_f32 v194, v162\n" \
  "v_exp_f32 v195, v163\n" \
  "v_exp_f32 v196, v164\n" \
  "v_cvt_pk_bf16_f32 v188, v192, v193\n" \
  "v_exp_f32 v197, v165\n" \
  "s_setprio 3\n" \
  "v_cvt_pk_bf16_f32 v189, v194, v195\n" \
  "v_exp_f32 v198, v166\n" \
  "v_exp_f32 v199, v167\n" \
  "v_cvt_pk_bf16_f32 v190, v196, v197\n" \
  "v_cvt_pk_bf16_f32 v191, v198, v199\n" \
  "s_waitcnt vmcnt(10)\n" \
  "v_mfma_f32_32x32x16_bf16 %0, v[216:219], v[184:187], %0\n" \
  "s_add_u32 s40, s40, 3584\n" \
  "s_addc_u32 s41, s41, 0\n" \
  "global_load_dwordx4 v[204:207], %14, s[40:41] offset:0 sc1\n" \
  "v_exp_f32 v192, v168\n" \
  "v_exp_f32 v193, v169\n" \
  "v_exp_f32 v194, v170\n" \
  "v_mfma_f32_32x32x16_bf16 v[152:167], v[212:215], %6, 0\n" \
  "global_load_dwordx4 v[208:211], %14, s[40:41] offset:1024 sc1\n" \
  "v_exp_f32 v195, v171\n" \
  "v_exp_f32 v196, v172\n" \
  "v_exp_f32 v197, v173\n" \
  "v_mfma_f32_32x32x16_bf16 %0, v[220:223], v[188:191], %0\n" \
  "v_exp_f32 v198, v174\n" \
  "v_cvt_pk_bf16_f32 v184, v192, v193\n" \
  "v_cvt_pk_bf16_f32 v185, v194, v195\n" \
  "v_exp_f32 v199, v175\n" \
  "v_mfma_f32_32x32x8bf16_1k v[152:167], v[250:251], %10, v[152:167]\n" \
  "v_cvt_pk_bf16_f32 v186, v196, v197\n" \
  "s_setprio 0\n" \
  "v_exp_f32 v192, v176\n" \
  "v_exp_f32 v193, v177\n" \
  "v_cvt_pk_bf16_f32 v187, v198, v199\n" \
  "v_exp_f32 v194, v178\n" \
  "v_exp_f32 v195, v179\n" \
  "v_exp_f32 v196, v180\n" \
  "v_cvt_pk_bf16_f32 v188, v192, v193\n" \
  "v_exp_f32 v197, v181\n" \
  "s_setprio 3\n" \
  "v_cvt_pk_bf16_f32 v189, v194, v195\n" \
  "v_exp_f32 v198, v182\n" \
  "v_exp_f32 v199, v183\n" \
  "v_cvt_pk_bf16_f32 v190, v196, v197\n" \
  "v_cvt_pk_bf16_f32 v191, v198, v199\n" \
  "v_mfma_f32_32x32x16_bf16 %1, v[216:219], v[184:187], %1\n" \
  "v_exp_f32 v192, v152\n" \
  "v_exp_f32 v193, v153\n" \
  "v_exp_f32 v194, v154\n" \
  "v_mfma_f32_32x32x16_bf16 v[168:183], v[212:215], %7, 0\n" \
  "v_exp_f32 v195, v155\n" \
  "v_exp_f32 v196, v156\n" \
  "v_exp_f32 v197, v157\n" \
  "v_mfma_f32_32x32x16_bf16 %1, v[220:223], v[188:191], %1\n" \
  "v_exp_f32 v198, v158\n" \
  "v_cvt_pk_bf16_f32 v184, v192, v193\n" \
  "v_cvt_pk_bf16_f32 v185, v194, v195\n" \
  "v_exp_f32 v199, v159\n" \
  "v_mfma_f32_32x32x8bf16_1k v[168:183], v[250:251], %11, v[168:183]\n" \
  "global_load_dwordx4 v[212:215], %12, s[40:41] offset:3584 sc1\n" \
  "v_cvt_pk_bf16_f32 v186, v196, v197\n" \
  "s_setprio 0\n" \
  "v_exp_f32 v192, v160\n" \
  "v_exp_f32 v193, v161\n" \
  "v_cvt_pk_bf16_f32 v187, v198, v199\n" \
  "v_exp_f32 v194, v162\n" \
  "v_exp_f32 v195, v163\n" \
  "v_exp_f32 v196, v164\n" \
  "v_cvt_pk_bf16_f32 v188, v192, v193\n" \
  "v_exp_f32 v197, v165\n" \
  "s_setprio 3\n" \
  "v_cvt_pk_bf16_f32 v189, v194, v195\n" \
  "v_exp_f32 v198, v166\n" \
  "v_exp_f32 v199, v167\n" \
  "v_cvt_pk_bf16_f32 v190, v196, v197\n" \
  "v_cvt_pk_bf16_f32 v191, v198, v199\n" \
  "global_load_dwordx2 v[250:251], %13, s[40:41] offset:3584 sc1\n" \
  "v_mfma_f32_32x32x16_bf16 %2, v[216:219], v[184:187], %2\n" \
  "v_exp_f32 v192, v168\n" \
  "v_exp_f32 v193, v169\n" \
  "v_exp_f32 v194, v170\n" \
  "s_waitcnt vmcnt(12)\n" \
  "v_mfma_f32_32x32x16_bf16 v[152:167], v[224:227], %4, 0\n" \
  "v_exp_f32 v195, v171\n" \
  "v_exp_f32 v196, v172\n" \
  "v_exp_f32 v197, v173\n" \
  "v_mfma_f32_32x32x16_bf16 %2, v[220:223], v[188:191], %2\n" \
  "v_exp_f32 v198, v174\n" \
  "v_cvt_pk_bf16_f32 v184, v192, v193\n" \
  "v_cvt_pk_bf16_f32 v185, v194, v195\n" \
  "v_exp_f32 v199, v175\n" \
  "v_mfma_f32_32x32x8bf16_1k v[152:167], v[252:253], %8, v[152:167]\n" \
  "v_cvt_pk_bf16_f32 v186, v196, v197\n" \
  "s_setprio 0\n" \
  "v_exp_f32 v192, v176\n" \
  "v_exp_f32 v193, v177\n" \
  "v_cvt_pk_bf16_f32 v187, v198, v199\n" \
  "v_exp_f32 v194, v178\n" \
  "v_exp_f32 v195, v179\n" \
  "v_exp_f32 v196, v180\n" \
  "v_cvt_pk_bf16_f32 v188, v192, v193\n" \
  "v_exp_f32 v197, v181\n" \
  "s_setprio 3\n" \
  "v_cvt_pk_bf16_f32 v189, v194, v195\n" \
  "v_exp_f32 v198, v182\n" \
  "v_exp_f32 v199, v183\n" \
  "v_cvt_pk_bf16_f32 v190, v196, v197\n" \
  "v_cvt_pk_bf16_f32 v191, v198, v199\n" \
  "v_mfma_f32_32x32x16_bf16 %3, v[216:219], v[184:187], %3\n" \
  "v_exp_f32 v192, v152\n" \
  "v_exp_f32 v193, v153\n" \
  "v_exp_f32 v194, v154\n" \
  "v_mfma_f32_32x32x16_bf16 v[168:183], v[224:227], %5, 0\n" \
  "v_exp_f32 v195, v155\n" \
  "v_exp_f32 v196, v156\n" \
  "v_exp_f32 v197, v157\n" \
  "v_mfma_f32_32x32x16_bf16 %3, v[220:223], v[188:191], %3\n" \
  "v_exp_f32 v198, v158\n" \
  "v_cvt_pk_bf16_f32 v184, v192, v193\n" \
  "v_cvt_pk_bf16_f32 v185, v194, v195\n" \
  "v_exp_f32 v199, v159\n" \
  "v_mfma_f32_32x32x8bf16_1k v[168:183], v[252:253], %9, v[168:183]\n" \
  "v_cvt_pk_bf16_f32 v186, v196, v197\n" \
  "s_setprio 0\n" \
  "v_exp_f32 v192, v160\n" \
  "v_exp_f32 v193, v161\n" \
  "v_cvt_pk_bf16_f32 v187, v198, v199\n" \
  "v_exp_f32 v194, v162\n" \
  "v_exp_f32 v195, v163\n" \
  "v_exp_f32 v196, v164\n" \
  "v_cvt_pk_bf16_f32 v188, v192, v193\n" \
  "v_exp_f32 v197, v165\n" \
  "s_setprio 3\n" \
  "v_cvt_pk_bf16_f32 v189, v194, v195\n" \
  "v_exp_f32 v198, v166\n" \
  "v_exp_f32 v199, v167\n" \
  "v_cvt_pk_bf16_f32 v190, v196, v197\n" \
  "v_cvt_pk_bf16_f32 v191, v198, v199\n" \
  "s_waitcnt vmcnt(10)\n" \
  "v_mfma_f32_32x32x16_bf16 %0, v[228:231], v[184:187], %0\n" \
  "s_add_u32 s40, s40, 3584\n" \
  "s_addc_u32 s41, s41, 0\n" \
  "global_load_dwordx4 v[216:219], %14, s[40:41] offset:0 sc1\n" \
  "v_exp_f32 v192, v168\n" \
  "v_exp_f32 v193, v169\n" \
  "v_exp_f32 v194, v170\n" \
  "v_mfma_f32_32x32x16_bf16 v[152:167], v[224:227], %6, 0\n" \
  "global_load_dwordx4 v[220:223], %14, s[40:41] offset:1024 sc1\n" \
  "v_exp_f32 v195, v171\n" \
  "v_exp_f32 v196, v172\n" \
  "v_exp_f32 v197, v173\n" \
  "v_mfma_f32_32x32x16_bf16 %0, v[232:235], v[188:191], %0\n" \
  "v_exp_f32 v198, v174\n" \
  "v_cvt_pk_bf16_f32 v184, v192, v193\n" \
  "v_cvt_pk_bf16_f32 v185, v194, v195\n" \
  "v_exp_f32 v199, v175\n" \
  "v_mfma_f32_32x32x8bf16_1k v[152:167], v[252:253], %10, v[152:167]\n" \
  "v_cvt_pk_bf16_f32 v186, v196, v197\n" \
  "s_setprio 0\n" \
  "v_exp_f32 v192, v176\n" \
  "v_exp_f32 v193, v177\n" \
  "v_cvt_pk_bf16_f32 v187, v198, v199\n" \
  "v_exp_f32 v194, v178\n" \
  "v_exp_f32 v195, v179\n" \
  "v_exp_f32 v196, v180\n" \
  "v_cvt_pk_bf16_f32 v188, v192, v193\n" \
  "v_exp_f32 v197, v181\n" \
  "s_setprio 3\n" \
  "v_cvt_pk_bf16_f32 v189, v194, v195\n" \
  "v_exp_f32 v198, v182\n" \
  "v_exp_f32 v199, v183\n" \
  "v_cvt_pk_bf16_f32 v190, v196, v197\n" \
  "v_cvt_pk_bf16_f32 v191, v198, v199\n" \
  "v_mfma_f32_32x32x16_bf16 %1, v[228:231], v[184:187], %1\n" \
  "v_exp_f32 v192, v152\n" \
  "v_exp_f32 v193, v153\n" \
  "v_exp_f32 v194, v154\n" \
  "v_mfma_f32_32x32x16_bf16 v[168:183], v[224:227], %7, 0\n" \
  "v_exp_f32 v195, v155\n" \
  "v_exp_f32 v196, v156\n" \
  "v_exp_f32 v197, v157\n" \
  "v_mfma_f32_32x32x16_bf16 %1, v[232:235], v[188:191], %1\n" \
  "v_exp_f32 v198, v158\n" \
  "v_cvt_pk_bf16_f32 v184, v192, v193\n" \
  "v_cvt_pk_bf16_f32 v185, v194, v195\n" \
  "v_exp_f32 v199, v159\n" \
  "v_mfma_f32_32x32x8bf16_1k v[168:183], v[252:253], %11, v[168:183]\n" \
  "global_load_dwordx4 v[224:227], %12, s[40:41] offset:3584 sc1\n" \
  "v_cvt_pk_bf16_f32 v186, v196, v197\n" \
  "s_setprio 0\n" \
  "v_exp_f32 v192, v160\n" \
  "v_exp_f32 v193, v161\n" \
  "v_cvt_pk_bf16_f32 v187, v198, v199\n" \
  "v_exp_f32 v194, v162\n" \
  "v_exp_f32 v195, v163\n" \
  "v_exp_f32 v196, v164\n" \
  "v_cvt_pk_bf16_f32 v188, v192, v193\n" \
  "v_exp_f32 v197, v165\n" \
  "s_setprio 3\n" \
  "v_cvt_pk_bf16_f32 v189, v194, v195\n" \
  "v_exp_f32 v198, v166\n" \
  "v_exp_f32 v199, v167\n" \
  "v_cvt_pk_bf16_f32 v190, v196, v197\n" \
  "v_cvt_pk_bf16_f32 v191, v198, v199\n" \
  "global_load_dwordx2 v[252:253], %13, s[40:41] offset:3584 sc1\n" \
  "v_mfma_f32_32x32x16_bf16 %2, v[228:231], v[184:187], %2\n" \
  "v_exp_f32 v192, v168\n" \
  "v_exp_f32 v193, v169\n" \
  "v_exp_f32 v194, v170\n" \
  "s_waitcnt vmcnt(12)\n" \
  "v_mfma_f32_32x32x16_bf16 v[152:167], v[236:239], %4, 0\n" \
  "v_exp_f32 v195, v171\n" \
  "v_exp_f32 v196, v172\n" \
  "v_exp_f32 v197, v173\n" \
  "v_mfma_f32_32x32x16_bf16 %2, v[232:235], v[188:191], %2\n" \
  "v_exp_f32 v198, v174\n" \
  "v_cvt_pk_bf16_f32 v184, v192, v193\n" \
  "v_cvt_pk_bf16_f32 v185, v194, v195\n" \
  "v_exp_f32 v199, v175\n" \
  "v_mfma_f32_32x32x8bf16_1k v[152:167], v[254:255], %8, v[152:167]\n" \
  "v_cvt_pk_bf16_f32 v186, v196, v197\n" \
  "s_setprio 0\n" \
  "v_exp_f32 v192, v176\n" \
  "v_exp_f32 v193, v177\n" \
  "v_cvt_pk_bf16_f32 v187, v198, v199\n" \
  "v_exp_f32 v194, v178\n" \
  "v_exp_f32 v195, v179\n" \
  "v_exp_f32 v196, v180\n" \
  "v_cvt_pk_bf16_f32 v188, v192, v193\n" \
  "v_exp_f32 v197, v181\n" \
  "s_setprio 3\n" \
  "v_cvt_pk_bf16_f32 v189, v194, v195\n" \
  "v_exp_f32 v198, v182\n" \
  "v_exp_f32 v199, v183\n" \
  "v_cvt_pk_bf16_f32 v190, v196, v197\n" \
  "v_cvt_pk_bf16_f32 v191, v198, v199\n" \
  "v_mfma_f32_32x32x16_bf16 %3, v[228:231], v[184:187], %3\n" \
  "v_exp_f32 v192, v152\n" \
  "v_exp_f32 v193, v153\n" \
  "v_exp_f32 v194, v154\n" \
  "v_mfma_f32_32x32x16_bf16 v[168:183], v[236:239], %5, 0\n" \
  "v_exp_f32 v195, v155\n" \
  "v_exp_f32 v196, v156\n" \
  "v_exp_f32 v197, v157\n" \
  "v_mfma_f32_32x32x16_bf16 %3, v[232:235], v[188:191], %3\n" \
  "v_exp_f32 v198, v158\n" \
  "v_cvt_pk_bf16_f32 v184, v192, v193\n" \
  "v_cvt_pk_bf16_f32 v185, v194, v195\n" \
  "v_exp_f32 v199, v159\n" \
  "v_mfma_f32_32x32x8bf16_1k v[168:183], v[254:255], %9, v[168:183]\n" \
  "v_cvt_pk_bf16_f32 v186, v196, v197\n" \
  "s_setprio 0\n" \
  "v_exp_f32 v192, v160\n" \
  "v_exp_f32 v193, v161\n" \
  "v_cvt_pk_bf16_f32 v187, v198, v199\n" \
  "v_exp_f32 v194, v162\n" \
  "v_exp_f32 v195, v163\n" \
  "v_exp_f32 v196, v164\n" \
  "v_cvt_pk_bf16_f32 v188, v192, v193\n" \
  "v_exp_f32 v197, v165\n" \
  "s_setprio 3\n" \
  "v_cvt_pk_bf16_f32 v189, v194, v195\n" \
  "v_exp_f32 v198, v166\n" \
  "v_exp_f32 v199, v167\n" \
  "v_cvt_pk_bf16_f32 v190, v196, v197\n" \
  "v_cvt_pk_bf16_f32 v191, v198, v199\n" \
  "s_waitcnt vmcnt(10)\n" \
  "v_mfma_f32_32x32x16_bf16 %0, v[240:243], v[184:187], %0\n" \
  "s_add_u32 s40, s40, 3584\n" \
  "s_addc_u32 s41, s41, 0\n" \
  "global_load_dwordx4 v[228:231], %14, s[40:41] offset:0 sc1\n" \
  "v_exp_f32 v192, v168\n" \
  "v_exp_f32 v193, v169\n" \
  "v_exp_f32 v194, v170\n" \
  "v_mfma_f32_32x32x16_bf16 v[152:167], v[236:239], %6, 0\n" \
  "global_load_dwordx4 v[232:235], %14, s[40:41] offset:1024 sc1\n" \
  "v_exp_f32 v195, v171\n" \
  "v_exp_f32 v196, v172\n" \
  "v_exp_f32 v197, v173\n" \
  "v_mfma_f32_32x32x16_bf16 %0, v[244:247], v[188:191], %0\n" \
  "v_exp_f32 v198, v174\n" \
  "v_cvt_pk_bf16_f32 v184, v192, v193\n" \
  "v_cvt_pk_bf16_f32 v185, v194, v195\n" \
  "v_exp_f32 v199, v175\n" \
  "v_mfma_f32_32x32x8bf16_1k v[152:167], v[254:255], %10, v[152:167]\n" \
  "v_cvt_pk_bf16_f32 v186, v196, v197\n" \
  "s_setprio 0\n" \
  "v_exp_f32 v192, v176\n" \
  "v_exp_f32 v193, v177\n" \
  "v_cvt_pk_bf16_f32 v187, v198, v199\n" \
  "v_exp_f32 v194, v178\n" \
  "v_exp_f32 v195, v179\n" \
  "v_exp_f32 v196, v180\n" \
  "v_cvt_pk_bf16_f32 v188, v192, v193\n" \
  "v_exp_f32 v197, v181\n" \
  "s_setprio 3\n" \
  "v_cvt_pk_bf16_f32 v189, v194, v195\n" \
  "v_exp_f32 v198, v182\n" \
  "v_exp_f32 v199, v183\n" \
  "v_cvt_pk_bf16_f32 v190, v196, v197\n" \
  "v_cvt_pk_bf16_f32 v191, v198, v199\n" \
  "v_mfma_f32_32x32x16_bf16 %1, v[240:243], v[184:187], %1\n" \
  "v_exp_f32 v192, v152\n" \
  "v_exp_f32 v193, v153\n" \
  "v_exp_f32 v194, v154\n" \
  "v_mfma_f32_32x32x16_bf16 v[168:183], v[236:239], %7, 0\n" \
  "v_exp_f32 v195, v155\n" \
  "v_exp_f32 v196, v156\n" \
  "v_exp_f32 v197, v157\n" \
  "v_mfma_f32_32x32x16_bf16 %1, v[244:247], v[188:191], %1\n" \
  "v_exp_f32 v198, v158\n" \
  "v_cvt_pk_bf16_f32 v184, v192, v193\n" \
  "v_cvt_pk_bf16_f32 v185, v194, v195\n" \
  "v_exp_f32 v199, v159\n" \
  "v_mfma_f32_32x32x8bf16_1k v[168:183], v[254:255], %11, v[168:183]\n" \
  "global_load_dwordx4 v[236:239], %12, s[40:41] offset:3584 sc1\n" \
  "v_cvt_pk_bf16_f32 v186, v196, v197\n" \
  "s_setprio 0\n" \
  "v_exp_f32 v192, v160\n" \
  "v_exp_f32 v193, v161\n" \
  "v_cvt_pk_bf16_f32 v187, v198, v199\n" \
  "v_exp_f32 v194, v162\n" \
  "v_exp_f32 v195, v163\n" \
  "v_exp_f32 v196, v164\n" \
  "v_cvt_pk_bf16_f32 v188, v192, v193\n" \
  "v_exp_f32 v197, v165\n" \
  "s_setprio 3\n" \
  "v_cvt_pk_bf16_f32 v189, v194, v195\n" \
  "v_exp_f32 v198, v166\n" \
  "v_exp_f32 v199, v167\n" \
  "v_cvt_pk_bf16_f32 v190, v196, v197\n" \
  "v_cvt_pk_bf16_f32 v191, v198, v199\n" \
  "global_load_dwordx2 v[254:255], %13, s[40:41] offset:3584 sc1\n" \
  "v_mfma_f32_32x32x16_bf16 %2, v[240:243], v[184:187], %2\n" \
  "v_exp_f32 v192, v168\n" \
  "v_exp_f32 v193, v169\n" \
  "v_exp_f32 v194, v170\n" \
  "s_waitcnt vmcnt(12)\n" \
  "v_mfma_f32_32x32x16_bf16 v[152:167], v[200:203], %4, 0\n" \
  "v_exp_f32 v195, v171\n" \
  "v_exp_f32 v196, v172\n" \
  "v_exp_f32 v197, v173\n" \
  "v_mfma_f32_32x32x16_bf16 %2, v[244:247], v[188:191], %2\n" \
  "v_exp_f32 v198, v174\n" \
  "v_cvt_pk_bf16_f32 v184, v192, v193\n" \
  "v_cvt_pk_bf16_f32 v185, v194, v195\n" \
  "v_exp_f32 v199, v175\n" \
  "v_mfma_f32_32x32x8bf16_1k v[152:167], v[248:249], %8, v[152:167]\n" \
  "v_cvt_pk_bf16_f32 v186, v196, v197\n" \
  "s_setprio 0\n" \
  "v_exp_f32 v192, v176\n" \
  "v_exp_f32 v193, v177\n" \
  "v_cvt_pk_bf16_f32 v187, v198, v199\n" \
  "v_exp_f32 v194, v178\n" \
  "v_exp_f32 v195, v179\n" \
  "v_exp_f32 v196, v180\n" \
  "v_cvt_pk_bf16_f32 v188, v192, v193\n" \
  "v_exp_f32 v197, v181\n" \
  "s_setprio 3\n" \
  "v_cvt_pk_bf16_f32 v189, v194, v195\n" \
  "v_exp_f32 v198, v182\n" \
  "v_exp_f32 v199, v183\n" \
  "v_cvt_pk_bf16_f32 v190, v196, v197\n" \
  "v_cvt_pk_bf16_f32 v191, v198, v199\n" \
  "s_sub_u32 s42, s42, 1\n" \
  "s_cmp_lg_u32 s42, 0\n" \
  "s_cbranch_scc1 1b\n" \
  "s_mov_b32 s42, 3\n" \
  "s_setprio 0\n" \
  "2:\n" \
  "v_mfma_f32_32x32x16_bf16 %3, v[240:243], v[184:187], %3\n" \
  "v_exp_f32 v192, v152\n" \
  "v_exp_f32 v193, v153\n" \
  "v_exp_f32 v194, v154\n" \
  "v_cvt_pk_bf16_f32 v184, v192, v193\n" \
  "v_exp_f32 v195, v155\n" \
  "v_mfma_f32_32x32x16_bf16 v[168:183], v[200:203], %5, 0\n" \
  "v_exp_f32 v196, v156\n" \
  "v_cvt_pk_bf16_f32 v185, v194, v195\n" \
  "v_exp_f32 v197, v157\n" \
  "v_exp_f32 v198, v158\n" \
  "v_exp_f32 v199, v159\n" \
  "v_mfma_f32_32x32x8bf16_1k v[168:183], v[248:249], %9, v[168:183]\n" \
  "v_exp_f32 v192, v160\n" \
  "v_cvt_pk_bf16_f32 v186, v196, v197\n" \
  "v_exp_f32 v193, v161\n" \
  "v_exp_f32 v194, v162\n" \
  "v_cvt_pk_bf16_f32 v187, v198, v199\n" \
  "v_exp_f32 v195, v163\n" \
  "v_mfma_f32_32x32x16_bf16 %3, v[244:247], v[188:191], %3\n" \
  "v_exp_f32 v196, v164\n" \
  "v_cvt_pk_bf16_f32 v188, v192, v193\n" \
  "v_exp_f32 v197, v165\n" \
  "v_exp_f32 v198, v166\n" \
  "v_cvt_pk_bf16_f32 v189, v194, v195\n" \
  "v_exp_f32 v199, v167\n" \
  "v_cvt_pk_bf16_f32 v190, v196, v197\n" \
  "v_cvt_pk_bf16_f32 v191, v198, v199\n" \
  "s_waitcnt vmcnt(10)\n" \
  "v_mfma_f32_32x32x16_bf16 %0, v[204:207], v[184:187], %0\n" \
  "s_add_u32 s40, s40, 3584\n" \
  "s_addc_u32 s41, s41, 0\n" \
  "global_load_dwordx4 v[240:243], %14, s[40:41] offset:0 sc1\n" \
  "v_exp_f32 v192, v168\n" \
  "v_exp_f32 v193, v169\n" \
  "v_exp_f32 v194, v170\n" \
  "v_cvt_pk_bf16_f32 v184, v192, v193\n" \
  "v_exp_f32 v195, v171\n" \
  "v_mfma_f32_32x32x16_bf16 v[152:167], v[200:203], %6, 0\n" \
  "global_load_dwordx4 v[244:247], %14, s[40:41] offset:1024 sc1\n" \
  "v_exp_f32 v196, v172\n" \
  "v_cvt_pk_bf16_f32 v185, v194, v195\n" \
  "v_exp_f32 v197, v173\n" \
  "v_exp_f32 v198, v174\n" \
  "v_exp_f32 v199, v175\n" \
  "v_mfma_f32_32x32x8bf16_1k v[152:167], v[248:249], %10, v[152:167]\n" \
  "v_exp_f32 v192, v176\n" \
  "v_cvt_pk_bf16_f32 v186, v196, v197\n" \
  "v_exp_f32 v193, v177\n" \
  "v_exp_f32 v194, v178\n" \
  "v_cvt_pk_bf16_f32 v187, v198, v199\n" \
  "v_exp_f32 v195, v179\n" \
  "v_mfma_f32_32x32x16_bf16 %0, v[208:211], v[188:191], %0\n" \
  "v_exp_f32 v196, v180\n" \
  "v_cvt_pk_bf16_f32 v188, v192, v193\n" \
  "v_exp_f32 v197, v181\n" \
  "v_exp_f32 v198, v182\n" \
  "v_cvt_pk_bf16_f32 v189, v194, v195\n" \
  "v_exp_f32 v199, v183\n" \
  "v_cvt_pk_bf16_f32 v190, v196, v197\n" \
  "v_cvt_pk_bf16_f32 v191, v198, v199\n" \
  "v_mfma_f32_32x32x16_bf16 %1, v[204:207], v[184:187], %1\n" \
  "v_exp_f32 v192, v152\n" \
  "v_exp_f32 v193, v153\n" \
  "v_exp_f32 v194, v154\n" \
  "v_cvt_pk_bf16_f32 v184, v192, v193\n" \
  "v_exp_f32 v195, v155\n" \
  "v_mfma_f32_32x32x16_bf16 v[168:183], v[200:203], %7, 0\n" \
  "v_exp_f32 v196, v156\n" \
  "v_cvt_pk_bf16_f32 v185, v194, v195\n" \
  "v_exp_f32 v197, v157\n" \
  "v_exp_f32 v198, v158\n" \
  "v_exp_f32 v199, v159\n" \
  "v_mfma_f32_32x32x8bf16_1k v[168:183], v[248:249], %11, v[168:183]\n" \
  "global_load_dwordx4 v[200:203], %12, s[40:41] offset:3584 sc1\n" \
  "v_exp_f32 v192, v160\n" \
  "v_cvt_pk_bf16_f32 v186, v196, v197\n" \
  "v_exp_f32 v193, v161\n" \
  "v_exp_f32 v194, v162\n" \
  "v_cvt_pk_bf16_f32 v187, v198, v199\n" \
  "v_exp_f32 v195, v163\n" \
  "v_mfma_f32_32x32x16_bf16 %1, v[208:211], v[188:191], %1\n" \
  "global_load_dwordx2 v[248:249], %13, s[40:41] offset:3584 sc1\n" \
  "v_exp_f32 v196, v164\n" \
  "v_cvt_pk_bf16_f32 v188, v192, v193\n" \
  "v_exp_f32 v197, v165\n" \
  "v_exp_f32 v198, v166\n" \
  "v_cvt_pk_bf16_f32 v189, v194, v195\n" \
  "v_exp_f32 v199, v167\n" \
  "v_cvt_pk_bf16_f32 v190, v196, v197\n" \
  "v_cvt_pk_bf16_f32 v191, v198, v199\n" \
  "v_mfma_f32_32x32x16_bf16 %2, v[204:207], v[184:187], %2\n" \
  "v_exp_f32 v192, v168\n" \
  "v_exp_f32 v193, v169\n" \
  "v_exp_f32 v194, v170\n" \
  "v_cvt_pk_bf16_f32 v184, v192, v193\n" \
  "v_exp_f32 v195, v171\n" \
  "s_waitcnt vmcnt(12)\n" \
  "v_mfma_f32_32x32x16_bf16 v[152:167], v[212:215], %4, 0\n" \
  "v_exp_f32 v196, v172\n" \
  "v_cvt_pk_bf16_f32 v185, v194, v195\n" \
  "v_exp_f32 v197, v173\n" \
  "v_exp_f32 v198, v174\n" \
  "v_exp_f32 v199, v175\n" \
  "v_mfma_f32_32x32x8bf16_1k v[152:167], v[250:251], %8, v[152:167]\n" \
  "v_exp_f32 v192, v176\n" \
  "v_cvt_pk_bf16_f32 v186, v196, v197\n" \
  "v_exp_f32 v193, v177\n" \
  "v_exp_f32 v194, v178\n" \
  "v_cvt_pk_bf16_f32 v187, v198, v199\n" \
  "v_exp_f32 v195, v179\n" \
  "v_mfma_f32_32x32x16_bf16 %2, v[208:211], v[188:191], %2\n" \
  "v_exp_f32 v196, v180\n" \
  "v_cvt_pk_bf16_f32 v188, v192, v193\n" \
  "v_exp_f32 v197, v181\n" \
  "v_exp_f32 v198, v182\n" \
  "v_cvt_pk_bf16_f32 v189, v194, v195\n" \
  "v_exp_f32 v199, v183\n" \
  "v_cvt_pk_bf16_f32 v190, v196, v197\n" \
  "v_cvt_pk_bf16_f32 v191, v198, v199\n" \
  "v_mfma_f32_32x32x16_bf16 %3, v[204:207], v[184:187], %3\n" \
  "v_exp_f32 v192, v152\n" \
  "v_exp_f32 v193, v153\n" \
  "v_exp_f32 v194, v154\n" \
  "v_cvt_pk_bf16_f32 v184, v192, v193\n" \
  "v_exp_f32 v195, v155\n" \
  "v_mfma_f32_32x32x16_bf16 v[168:183], v[212:215], %5, 0\n" \
  "v_exp_f32 v196, v156\n" \
  "v_cvt_pk_bf16_f32 v185, v194, v195\n" \
  "v_exp_f32 v197, v157\n" \
  "v_exp_f32 v198, v158\n" \
  "v_exp_f32 v199, v159\n" \
  "v_mfma_f32_32x32x8bf16_1k v[168:183], v[250:251], %9, v[168:183]\n" \
  "v_exp_f32 v192, v160\n" \
  "v_cvt_pk_bf16_f32 v186, v196, v197\n" \
  "v_exp_f32 v193, v161\n" \
  "v_exp_f32 v194, v162\n" \
  "v_cvt_pk_bf16_f32 v187, v198, v199\n" \
  "v_exp_f32 v195, v163\n" \
  "v_mfma_f32_32x32x16_bf16 %3, v[208:211], v[188:191], %3\n" \
  "v_exp_f32 v196, v164\n" \
  "v_cvt_pk_bf16_f32 v188, v192, v193\n" \
  "v_exp_f32 v197, v165\n" \
  "v_exp_f32 v198, v166\n" \
  "v_cvt_pk_bf16_f32 v189, v194, v195\n" \
  "v_exp_f32 v199, v167\n" \
  "v_cvt_pk_bf16_f32 v190, v196, v197\n" \
  "v_cvt_pk_bf16_f32 v191, v198, v199\n" \
  "s_waitcnt vmcnt(10)\n" \
  "v_mfma_f32_32x32x16_bf16 %0, v[216:219], v[184:187], %0\n" \
  "s_add_u32 s40, s40, 3584\n" \
  "s_addc_u32 s41, s41, 0\n" \
  "global_load_dwordx4 v[204:207], %14, s[40:41] offset:0 sc1\n" \
  "v_exp_f32 v192, v168\n" \
  "v_exp_f32 v193, v169\n" \
  "v_exp_f32 v194, v170\n" \
  "v_cvt_pk_bf16_f32 v184, v192, v193\n" \
  "v_exp_f32 v195, v171\n" \
  "v_mfma_f32_32x32x16_bf16 v[152:167], v[212:215], %6, 0\n" \
  "global_load_dwordx4 v[208:211], %14, s[40:41] offset:1024 sc1\n" \
  "v_exp_f32 v196, v172\n" \
  "v_cvt_pk_bf16_f32 v185, v194, v195\n" \
  "v_exp_f32 v197, v173\n" \
  "v_exp_f32 v198, v174\n" \
  "v_exp_f32 v199, v175\n" \
  "v_mfma_f32_32x32x8bf16_1k v[152:167], v[250:251], %10, v[152:167]\n" \
  "v_exp_f32 v192, v176\n" \
  "v_cvt_pk_bf16_f32 v186, v196, v197\n" \
  "v_exp_f32 v193, v177\n" \
  "v_exp_f32 v194, v178\n" \
  "v_cvt_pk_bf16_f32 v187, v198, v199\n" \
  "v_exp_f32 v195, v179\n" \
  "v_mfma_f32_32x32x16_bf16 %0, v[220:223], v[188:191], %0\n" \
  "v_exp_f32 v196, v180\n" \
  "v_cvt_pk_bf16_f32 v188, v192, v193\n" \
  "v_exp_f32 v197, v181\n" \
  "v_exp_f32 v198, v182\n" \
  "v_cvt_pk_bf16_f32 v189, v194, v195\n" \
  "v_exp_f32 v199, v183\n" \
  "v_cvt_pk_bf16_f32 v190, v196, v197\n" \
  "v_cvt_pk_bf16_f32 v191, v198, v199\n" \
  "v_mfma_f32_32x32x16_bf16 %1, v[216:219], v[184:187], %1\n" \
  "v_exp_f32 v192, v152\n" \
  "v_exp_f32 v193, v153\n" \
  "v_exp_f32 v194, v154\n" \
  "v_cvt_pk_bf16_f32 v184, v192, v193\n" \
  "v_exp_f32 v195, v155\n" \
  "v_mfma_f32_32x32x16_bf16 v[168:183], v[212:215], %7, 0\n" \
  "v_exp_f32 v196, v156\n" \
  "v_cvt_pk_bf16_f32 v185, v194, v195\n" \
  "v_exp_f32 v197, v157\n" \
  "v_exp_f32 v198, v158\n" \
  "v_exp_f32 v199, v159\n" \
  "v_mfma_f32_32x32x8bf16_1k v[168:183], v[250:251], %11, v[168:183]\n" \
  "global_load_dwordx4 v[212:215], %12, s[40:41] offset:3584 sc1\n" \
  "v_exp_f32 v192, v160\n" \
  "v_cvt_pk_bf16_f32 v186, v196, v197\n" \
  "v_exp_f32 v193, v161\n" \
  "v_exp_f32 v194, v162\n" \
  "v_cvt_pk_bf16_f32 v187, v198, v199\n" \
  "v_exp_f32 v195, v163\n" \
  "v_mfma_f32_32x32x16_bf16 %1, v[220:223], v[188:191], %1\n" \
  "global_load_dwordx2 v[250:251], %13, s[40:41] offset:3584 sc1\n" \
  "v_exp_f32 v196, v164\n" \
  "v_cvt_pk_bf16_f32 v188, v192, v193\n" \
  "v_exp_f32 v197, v165\n" \
  "v_exp_f32 v198, v166\n" \
  "v_cvt_pk_bf16_f32 v189, v194, v195\n" \
  "v_exp_f32 v199, v167\n" \
  "v_cvt_pk_bf16_f32 v190, v196, v197\n" \
  "v_cvt_pk_bf16_f32 v191, v198, v199\n" \
  "v_mfma_f32_32x32x16_bf16 %2, v[216:219], v[184:187], %2\n" \
  "v_exp_f32 v192, v168\n" \
  "v_exp_f32 v193, v169\n" \
  "v_exp_f32 v194, v170\n" \
  "v_cvt_pk_bf16_f32 v184, v192, v193\n" \
  "v_exp_f32 v195, v171\n" \
  "s_waitcnt vmcnt(12)\n" \
  "v_mfma_f32_32x32x16_bf16 v[152:167], v[224:227], %4, 0\n" \
  "v_exp_f32 v196, v172\n" \
  "v_cvt_pk_bf16_f32 v185, v194, v195\n" \
  "v_exp_f32 v197, v173\n" \
  "v_exp_f32 v198, v174\n" \
  "v_exp_f32 v199, v175\n" \
  "v_mfma_f32_32x32x8bf16_1k v[152:167], v[252:253], %8, v[152:167]\n" \
  "v_exp_f32 v192, v176\n" \
  "v_cvt_pk_bf16_f32 v186, v196, v197\n" \
  "v_exp_f32 v193, v177\n" \
  "v_exp_f32 v194, v178\n" \
  "v_cvt_pk_bf16_f32 v187, v198, v199\n" \
  "v_exp_f32 v195, v179\n" \
  "v_mfma_f32_32x32x16_bf16 %2, v[220:223], v[188:191], %2\n" \
  "v_exp_f32 v196, v180\n" \
  "v_cvt_pk_bf16_f32 v188, v192, v193\n" \
  "v_exp_f32 v197, v181\n" \
  "v_exp_f32 v198, v182\n" \
  "v_cvt_pk_bf16_f32 v189, v194, v195\n" \
  "v_exp_f32 v199, v183\n" \
  "v_cvt_pk_bf16_f32 v190, v196, v197\n" \
  "v_cvt_pk_bf16_f32 v191, v198, v199\n" \
  "v_mfma_f32_32x32x16_bf16 %3, v[216:219], v[184:187], %3\n" \
  "v_exp_f32 v192, v152\n" \
  "v_exp_f32 v193, v153\n" \
  "v_exp_f32 v194, v154\n" \
  "v_cvt_pk_bf16_f32 v184, v192, v193\n" \
  "v_exp_f32 v195, v155\n" \
  "v_mfma_f32_32x32x16_bf16 v[168:183], v[224:227], %5, 0\n" \
  "v_exp_f32 v196, v156\n" \
  "v_cvt_pk_bf16_f32 v185, v194, v195\n" \
  "v_exp_f32 v197, v157\n" \
  "v_exp_f32 v198, v158\n" \
  "v_exp_f32 v199, v159\n" \
  "v_mfma_f32_32x32x8bf16_1k v[168:183], v[252:253], %9, v[168:183]\n" \
  "v_exp_f32 v192, v160\n" \
  "v_cvt_pk_bf16_f32 v186, v196, v197\n" \
  "v_exp_f32 v193, v161\n" \
  "v_exp_f32 v194, v162\n" \
  "v_cvt_pk_bf16_f32 v187, v198, v199\n" \
  "v_exp_f32 v195, v163\n" \
  "v_mfma_f32_32x32x16_bf16 %3, v[220:223], v[188:191], %3\n" \
  "v_exp_f32 v196, v164\n" \
  "v_cvt_pk_bf16_f32 v188, v192, v193\n" \
  "v_exp_f32 v197, v165\n" \
  "v_exp_f32 v198, v166\n" \
  "v_cvt_pk_bf16_f32 v189, v194, v195\n" \
  "v_exp_f32 v199, v167\n" \
  "v_cvt_pk_bf16_f32 v190, v196, v197\n" \
  "v_cvt_pk_bf16_f32 v191, v198, v199\n" \
  "s_waitcnt vmcnt(10)\n" \
  "v_mfma_f32_32x32x16_bf16 %0, v[228:231], v[184:187], %0\n" \
  "s_add_u32 s40, s40, 3584\n" \
  "s_addc_u32 s41, s41, 0\n" \
  "global_load_dwordx4 v[216:219], %14, s[40:41] offset:0 sc1\n" \
  "v_exp_f32 v192, v168\n" \
  "v_exp_f32 v193, v169\n" \
  "v_exp_f32 v194, v170\n" \
  "v_cvt_pk_bf16_f32 v184, v192, v193\n" \
  "v_exp_f32 v195, v171\n" \
  "v_mfma_f32_32x32x16_bf16 v[152:167], v[224:227], %6, 0\n" \
  "global_load_dwordx4 v[220:223], %14, s[40:41] offset:1024 sc1\n" \
  "v_exp_f32 v196, v172\n" \
  "v_cvt_pk_bf16_f32 v185, v194, v195\n" \
  "v_exp_f32 v197, v173\n" \
  "v_exp_f32 v198, v174\n" \
  "v_exp_f32 v199, v175\n" \
  "v_mfma_f32_32x32x8bf16_1k v[152:167], v[252:253], %10, v[152:167]\n" \
  "v_exp_f32 v192, v176\n" \
  "v_cvt_pk_bf16_f32 v186, v196, v197\n" \
  "v_exp_f32 v193, v177\n" \
  "v_exp_f32 v194, v178\n" \
  "v_cvt_pk_bf16_f32 v187, v198, v199\n" \
  "v_exp_f32 v195, v179\n" \
  "v_mfma_f32_32x32x16_bf16 %0, v[232:235], v[188:191], %0\n" \
  "v_exp_f32 v196, v180\n" \
  "v_cvt_pk_bf16_f32 v188, v192, v193\n" \
  "v_exp_f32 v197, v181\n" \
  "v_exp_f32 v198, v182\n" \
  "v_cvt_pk_bf16_f32 v189, v194, v195\n" \
  "v_exp_f32 v199, v183\n" \
  "v_cvt_pk_bf16_f32 v190, v196, v197\n" \
  "v_cvt_pk_bf16_f32 v191, v198, v199\n" \
  "v_mfma_f32_32x32x16_bf16 %1, v[228:231], v[184:187], %1\n" \
  "v_exp_f32 v192, v152\n" \
  "v_exp_f32 v193, v153\n" \
  "v_exp_f32 v194, v154\n" \
  "v_cvt_pk_bf16_f32 v184, v192, v193\n" \
  "v_exp_f32 v195, v155\n" \
  "v_mfma_f32_32x32x16_bf16 v[168:183], v[224:227], %7, 0\n" \
  "v_exp_f32 v196, v156\n" \
  "v_cvt_pk_bf16_f32 v185, v194, v195\n" \
  "v_exp_f32 v197, v157\n" \
  "v_exp_f32 v198, v158\n" \
  "v_exp_f32 v199, v159\n" \
  "v_mfma_f32_32x32x8bf16_1k v[168:183], v[252:253], %11, v[168:183]\n" \
  "global_load_dwordx4 v[224:227], %12, s[40:41] offset:3584 sc1\n" \
  "v_exp_f32 v192, v160\n" \
  "v_cvt_pk_bf16_f32 v186, v196, v197\n" \
  "v_exp_f32 v193, v161\n" \
  "v_exp_f32 v194, v162\n" \
  "v_cvt_pk_bf16_f32 v187, v198, v199\n" \
  "v_exp_f32 v195, v163\n" \
  "v_mfma_f32_32x32x16_bf16 %1, v[232:235], v[188:191], %1\n" \
  "global_load_dwordx2 v[252:253], %13, s[40:41] offset:3584 sc1\n" \
  "v_exp_f32 v196, v164\n" \
  "v_cvt_pk_bf16_f32 v188, v192, v193\n" \
  "v_exp_f32 v197, v165\n" \
  "v_exp_f32 v198, v166\n" \
  "v_cvt_pk_bf16_f32 v189, v194, v195\n" \
  "v_exp_f32 v199, v167\n" \
  "v_cvt_pk_bf16_f32 v190, v196, v197\n" \
  "v_cvt_pk_bf16_f32 v191, v198, v199\n" \
  "v_mfma_f32_32x32x16_bf16 %2, v[228:231], v[184:187], %2\n" \
  "v_exp_f32 v192, v168\n" \
  "v_exp_f32 v193, v169\n" \
  "v_exp_f32 v194, v170\n" \
  "v_cvt_pk_bf16_f32 v184, v192, v193\n" \
  "v_exp_f32 v195, v171\n" \
  "s_waitcnt vmcnt(12)\n" \
  "v_mfma_f32_32x32x16_bf16 v[152:167], v[236:239], %4, 0\n" \
  "v_exp_f32 v196, v172\n" \
  "v_cvt_pk_bf16_f32 v185, v194, v195\n" \
  "v_exp_f32 v197, v173\n" \
  "v_exp_f32 v198, v174\n" \
  "v_exp_f32 v199, v175\n" \
  "v_mfma_f32_32x32x8bf16_1k v[152:167], v[254:255], %8, v[152:167]\n" \
  "v_exp_f32 v192, v176\n" \
  "v_cvt_pk_bf16_f32 v186, v196, v197\n" \
  "v_exp_f32 v193, v177\n" \
  "v_exp_f32 v194, v178\n" \
  "v_cvt_pk_bf16_f32 v187, v198, v199\n" \
  "v_exp_f32 v195, v179\n" \
  "v_mfma_f32_32x32x16_bf16 %2, v[232:235], v[188:191], %2\n" \
  "v_exp_f32 v196, v180\n" \
  "v_cvt_pk_bf16_f32 v188, v192, v193\n" \
  "v_exp_f32 v197, v181\n" \
  "v_exp_f32 v198, v182\n" \
  "v_cvt_pk_bf16_f32 v189, v194, v195\n" \
  "v_exp_f32 v199, v183\n" \
  "v_cvt_pk_bf16_f32 v190, v196, v197\n" \
  "v_cvt_pk_bf16_f32 v191, v198, v199\n" \
  "v_mfma_f32_32x32x16_bf16 %3, v[228:231], v[184:187], %3\n" \
  "v_exp_f32 v192, v152\n" \
  "v_exp_f32 v193, v153\n" \
  "v_exp_f32 v194, v154\n" \
  "v_cvt_pk_bf16_f32 v184, v192, v193\n" \
  "v_exp_f32 v195, v155\n" \
  "v_mfma_f32_32x32x16_bf16 v[168:183], v[236:239], %5, 0\n" \
  "v_exp_f32 v196, v156\n" \
  "v_cvt_pk_bf16_f32 v185, v194, v195\n" \
  "v_exp_f32 v197, v157\n" \
  "v_exp_f32 v198, v158\n" \
  "v_exp_f32 v199, v159\n" \
  "v_mfma_f32_32x32x8bf16_1k v[168:183], v[254:255], %9, v[168:183]\n" \
  "v_exp_f32 v192, v160\n" \
  "v_cvt_pk_bf16_f32 v186, v196, v197\n" \
  "v_exp_f32 v193, v161\n" \
  "v_exp_f32 v194, v162\n" \
  "v_cvt_pk_bf16_f32 v187, v198, v199\n" \
  "v_exp_f32 v195, v163\n" \
  "v_mfma_f32_32x32x16_bf16 %3, v[232:235], v[188:191], %3\n" \
  "v_exp_f32 v196, v164\n" \
  "v_cvt_pk_bf16_f32 v188, v192, v193\n" \
  "v_exp_f32 v197, v165\n" \
  "v_exp_f32 v198, v166\n" \
  "v_cvt_pk_bf16_f32 v189, v194, v195\n" \
  "v_exp_f32 v199, v167\n" \
  "v_cvt_pk_bf16_f32 v190, v196, v197\n" \
  "v_cvt_pk_bf16_f32 v191, v198, v199\n" \
  "s_waitcnt vmcnt(10)\n" \
  "v_mfma_f32_32x32x16_bf16 %0, v[240:243], v[184:187], %0\n" \
  "s_add_u32 s40, s40, 3584\n" \
  "s_addc_u32 s41, s41, 0\n" \
  "global_load_dwordx4 v[228:231], %14, s[40:41] offset:0 sc1\n" \
  "v_exp_f32 v192, v168\n" \
  "v_exp_f32 v193, v169\n" \
  "v_exp_f32 v194, v170\n" \
  "v_cvt_pk_bf16_f32 v184, v192, v193\n" \
  "v_exp_f32 v195, v171\n" \
  "v_mfma_f32_32x32x16_bf16 v[152:167], v[236:239], %6, 0\n" \
  "global_load_dwordx4 v[232:235], %14, s[40:41] offset:1024 sc1\n" \
  "v_exp_f32 v196, v172\n" \
  "v_cvt_pk_bf16_f32 v185, v194, v195\n" \
  "v_exp_f32 v197, v173\n" \
  "v_exp_f32 v198, v174\n" \
  "v_exp_f32 v199, v175\n" \
  "v_mfma_f32_32x32x8bf16_1k v[152:167], v[254:255], %10, v[152:167]\n" \
  "v_exp_f32 v192, v176\n" \
  "v_cvt_pk_bf16_f32 v186, v196, v197\n" \
  "v_exp_f32 v193, v177\n" \
  "v_exp_f32 v194, v178\n" \
  "v_cvt_pk_bf16_f32 v187, v198, v199\n" \
  "v_exp_f32 v195, v179\n" \
  "v_mfma_f32_32x32x16_bf16 %0, v[244:247], v[188:191], %0\n" \
  "v_exp_f32 v196, v180\n" \
  "v_cvt_pk_bf16_f32 v188, v192, v193\n" \
  "v_exp_f32 v197, v181\n" \
  "v_exp_f32 v198, v182\n" \
  "v_cvt_pk_bf16_f32 v189, v194, v195\n" \
  "v_exp_f32 v199, v183\n" \
  "v_cvt_pk_bf16_f32 v190, v196, v197\n" \
  "v_cvt_pk_bf16_f32 v191, v198, v199\n" \
  "v_mfma_f32_32x32x16_bf16 %1, v[240:243], v[184:187], %1\n" \
  "v_exp_f32 v192, v152\n" \
  "v_exp_f32 v193, v153\n" \
  "v_exp_f32 v194, v154\n" \
  "v_cvt_pk_bf16_f32 v184, v192, v193\n" \
  "v_exp_f32 v195, v155\n" \
  "v_mfma_f32_32x32x16_bf16 v[168:183], v[236:239], %7, 0\n" \
  "v_exp_f32 v196, v156\n" \
  "v_cvt_pk_bf16_f32 v185, v194, v195\n" \
  "v_exp_f32 v197, v157\n" \
  "v_exp_f32 v198, v158\n" \
  "v_exp_f32 v199, v159\n" \
  "v_mfma_f32_32x32x8bf16_1k v[168:183], v[254:255], %11, v[168:183]\n" \
  "global_load_dwordx4 v[236:239], %12, s[40:41] offset:3584 sc1\n" \
  "v_exp_f32 v192, v160\n" \
  "v_cvt_pk_bf16_f32 v186, v196, v197\n" \
  "v_exp_f32 v193, v161\n" \
  "v_exp_f32 v194, v162\n" \
  "v_cvt_pk_bf16_f32 v187, v198, v199\n" \
  "v_exp_f32 v195, v163\n" \
  "v_mfma_f32_32x32x16_bf16 %1, v[244:247], v[188:191], %1\n" \
  "global_load_dwordx2 v[254:255], %13, s[40:41] offset:3584 sc1\n" \
  "v_exp_f32 v196, v164\n" \
  "v_cvt_pk_bf16_f32 v188, v192, v193\n" \
  "v_exp_f32 v197, v165\n" \
  "v_exp_f32 v198, v166\n" \
  "v_cvt_pk_bf16_f32 v189, v194, v195\n" \
  "v_exp_f32 v199, v167\n" \
  "v_cvt_pk_bf16_f32 v190, v196, v197\n" \
  "v_cvt_pk_bf16_f32 v191, v198, v199\n" \
  "v_mfma_f32_32x32x16_bf16 %2, v[240:243], v[184:187], %2\n" \
  "v_exp_f32 v192, v168\n" \
  "v_exp_f32 v193, v169\n" \
  "v_exp_f32 v194, v170\n" \
  "v_cvt_pk_bf16_f32 v184, v192, v193\n" \
  "v_exp_f32 v195, v171\n" \
  "s_waitcnt vmcnt(12)\n" \
  "v_mfma_f32_32x32x16_bf16 v[152:167], v[200:203], %4, 0\n" \
  "v_exp_f32 v196, v172\n" \
  "v_cvt_pk_bf16_f32 v185, v194, v195\n" \
  "v_exp_f32 v197, v173\n" \
  "v_exp_f32 v198, v174\n" \
  "v_exp_f32 v199, v175\n" \
  "v_mfma_f32_32x32x8bf16_1k v[152:167], v[248:249], %8, v[152:167]\n" \
  "v_exp_f32 v192, v176\n" \
  "v_cvt_pk_bf16_f32 v186, v196, v197\n" \
  "v_exp_f32 v193, v177\n" \
  "v_exp_f32 v194, v178\n" \
  "v_cvt_pk_bf16_f32 v187, v198, v199\n" \
  "v_exp_f32 v195, v179\n" \
  "v_mfma_f32_32x32x16_bf16 %2, v[244:247], v[188:191], %2\n" \
  "v_exp_f32 v196, v180\n" \
  "v_cvt_pk_bf16_f32 v188, v192, v193\n" \
  "v_exp_f32 v197, v181\n" \
  "v_exp_f32 v198, v182\n" \
  "v_cvt_pk_bf16_f32 v189, v194, v195\n" \
  "v_exp_f32 v199, v183\n" \
  "v_cvt_pk_bf16_f32 v190, v196, v197\n" \
  "v_cvt_pk_bf16_f32 v191, v198, v199\n" \
  "s_sub_u32 s42, s42, 1\n" \
  "s_cmp_lg_u32 s42, 0\n" \
  "s_cbranch_scc1 2b\n" \
  "v_mfma_f32_32x32x16_bf16 %3, v[240:243], v[184:187], %3\n" \
  "v_exp_f32 v192, v152\n" \
  "v_exp_f32 v193, v153\n" \
  "v_exp_f32 v194, v154\n" \
  "v_cvt_pk_bf16_f32 v184, v192, v193\n" \
  "v_exp_f32 v195, v155\n" \
  "v_mfma_f32_32x32x16_bf16 v[168:183], v[200:203], %5, 0\n" \
  "v_exp_f32 v196, v156\n" \
  "v_cvt_pk_bf16_f32 v185, v194, v195\n" \
  "v_exp_f32 v197, v157\n" \
  "v_exp_f32 v198, v158\n" \
  "v_exp_f32 v199, v159\n" \
  "v_mfma_f32_32x32x8bf16_1k v[168:183], v[248:249], %9, v[168:183]\n" \
  "v_exp_f32 v192, v160\n" \
  "v_cvt_pk_bf16_f32 v186, v196, v197\n" \
  "v_exp_f32 v193, v161\n" \
  "v_exp_f32 v194, v162\n" \
  "v_cvt_pk_bf16_f32 v187, v198, v199\n" \
  "v_exp_f32 v195, v163\n" \
  "v_mfma_f32_32x32x16_bf16 %3, v[244:247], v[188:191], %3\n" \
  "v_exp_f32 v196, v164\n" \
  "v_cvt_pk_bf16_f32 v188, v192, v193\n" \
  "v_exp_f32 v197, v165\n" \
  "v_exp_f32 v198, v166\n" \
  "v_cvt_pk_bf16_f32 v189, v194, v195\n" \
  "v_exp_f32 v199, v167\n" \
  "v_cvt_pk_bf16_f32 v190, v196, v197\n" \
  "v_cvt_pk_bf16_f32 v191, v198, v199\n" \
  "s_waitcnt vmcnt(10)\n" \
  "v_mfma_f32_32x32x16_bf16 %0, v[204:207], v[184:187], %0\n" \
  "s_add_u32 s40, s40, 3584\n" \
  "s_addc_u32 s41, s41, 0\n" \
  "global_load_dwordx4 v[240:243], %14, s[40:41] offset:0 sc1\n" \
  "v_exp_f32 v192, v168\n" \
  "v_exp_f32 v193, v169\n" \
  "v_exp_f32 v194, v170\n" \
  "v_cvt_pk_bf16_f32 v184, v192, v193\n" \
  "v_exp_f32 v195, v171\n" \
  "v_mfma_f32_32x32x16_bf16 v[152:167], v[200:203], %6, 0\n" \
  "global_load_dwordx4 v[244:247], %14, s[40:41] offset:1024 sc1\n" \
  "v_exp_f32 v196, v172\n" \
  "v_cvt_pk_bf16_f32 v185, v194, v195\n" \
  "v_exp_f32 v197, v173\n" \
  "v_exp_f32 v198, v174\n" \
  "v_exp_f32 v199, v175\n" \
  "v_mfma_f32_32x32x8bf16_1k v[152:167], v[248:249], %10, v[152:167]\n" \
  "v_exp_f32 v192, v176\n" \
  "v_cvt_pk_bf16_f32 v186, v196, v197\n" \
  "v_exp_f32 v193, v177\n" \
  "v_exp_f32 v194, v178\n" \
  "v_cvt_pk_bf16_f32 v187, v198, v199\n" \
  "v_exp_f32 v195, v179\n" \
  "v_mfma_f32_32x32x16_bf16 %0, v[208:211], v[188:191], %0\n" \
  "v_exp_f32 v196, v180\n" \
  "v_cvt_pk_bf16_f32 v188, v192, v193\n" \
  "v_exp_f32 v197, v181\n" \
  "v_exp_f32 v198, v182\n" \
  "v_cvt_pk_bf16_f32 v189, v194, v195\n" \
  "v_exp_f32 v199, v183\n" \
  "v_cvt_pk_bf16_f32 v190, v196, v197\n" \
  "v_cvt_pk_bf16_f32 v191, v198, v199\n" \
  "v_mfma_f32_32x32x16_bf16 %1, v[204:207], v[184:187], %1\n" \
  "v_exp_f32 v192, v152\n" \
  "v_exp_f32 v193, v153\n" \
  "v_exp_f32 v194, v154\n" \
  "v_cvt_pk_bf16_f32 v184, v192, v193\n" \
  "v_exp_f32 v195, v155\n" \
  "v_mfma_f32_32x32x16_bf16 v[168:183], v[200:203], %7, 0\n" \
  "v_exp_f32 v196, v156\n" \
  "v_cvt_pk_bf16_f32 v185, v194, v195\n" \
  "v_exp_f32 v197, v157\n" \
  "v_exp_f32 v198, v158\n" \
  "v_exp_f32 v199, v159\n" \
  "v_mfma_f32_32x32x8bf16_1k v[168:183], v[248:249], %11, v[168:183]\n" \
  "v_exp_f32 v192, v160\n" \
  "v_cvt_pk_bf16_f32 v186, v196, v197\n" \
  "v_exp_f32 v193, v161\n" \
  "v_exp_f32 v194, v162\n" \
  "v_cvt_pk_bf16_f32 v187, v198, v199\n" \
  "v_exp_f32 v195, v163\n" \
  "v_mfma_f32_32x32x16_bf16 %1, v[208:211], v[188:191], %1\n" \
  "v_exp_f32 v196, v164\n" \
  "v_cvt_pk_bf16_f32 v188, v192, v193\n" \
  "v_exp_f32 v197, v165\n" \
  "v_exp_f32 v198, v166\n" \
  "v_cvt_pk_bf16_f32 v189, v194, v195\n" \
  "v_exp_f32 v199, v167\n" \
  "v_cvt_pk_bf16_f32 v190, v196, v197\n" \
  "v_cvt_pk_bf16_f32 v191, v198, v199\n" \
  "v_mfma_f32_32x32x16_bf16 %2, v[204:207], v[184:187], %2\n" \
  "v_exp_f32 v192, v168\n" \
  "v_exp_f32 v193, v169\n" \
  "v_exp_f32 v194, v170\n" \
  "v_cvt_pk_bf16_f32 v184, v192, v193\n" \
  "v_exp_f32 v195, v171\n" \
  "s_waitcnt vmcnt(10)\n" \
  "v_mfma_f32_32x32x16_bf16 v[152:167], v[212:215], %4, 0\n" \
  "v_exp_f32 v196, v172\n" \
  "v_cvt_pk_bf16_f32 v185, v194, v195\n" \
  "v_exp_f32 v197, v173\n" \
  "v_exp_f32 v198, v174\n" \
  "v_exp_f32 v199, v175\n" \
  "v_mfma_f32_32x32x8bf16_1k v[152:167], v[250:251], %8, v[152:167]\n" \
  "v_exp_f32 v192, v176\n" \
  "v_cvt_pk_bf16_f32 v186, v196, v197\n" \
  "v_exp_f32 v193, v177\n" \
  "v_exp_f32 v194, v178\n" \
  "v_cvt_pk_bf16_f32 v187, v198, v199\n" \
  "v_exp_f32 v195, v179\n" \
  "v_mfma_f32_32x32x16_bf16 %2, v[208:211], v[188:191], %2\n" \
  "v_exp_f32 v196, v180\n" \
  "v_cvt_pk_bf16_f32 v188, v192, v193\n" \
  "v_exp_f32 v197, v181\n" \
  "v_exp_f32 v198, v182\n" \
  "v_cvt_pk_bf16_f32 v189, v194, v195\n" \
  "v_exp_f32 v199, v183\n" \
  "v_cvt_pk_bf16_f32 v190, v196, v197\n" \
  "v_cvt_pk_bf16_f32 v191, v198, v199\n" \
  "v_mfma_f32_32x32x16_bf16 %3, v[204:207], v[184:187], %3\n" \
  "v_exp_f32 v192, v152\n" \
  "v_exp_f32 v193, v153\n" \
  "v_exp_f32 v194, v154\n" \
  "v_cvt_pk_bf16_f32 v184, v192, v193\n" \
  "v_exp_f32 v195, v155\n" \
  "v_mfma_f32_32x32x16_bf16 v[168:183], v[212:215], %5, 0\n" \
  "v_exp_f32 v196, v156\n" \
  "v_cvt_pk_bf16_f32 v185, v194, v195\n" \
  "v_exp_f32 v197, v157\n" \
  "v_exp_f32 v198, v158\n" \
  "v_exp_f32 v199, v159\n" \
  "v_mfma_f32_32x32x8bf16_1k v[168:183], v[250:251], %9, v[168:183]\n" \
  "v_exp_f32 v192, v160\n" \
  "v_cvt_pk_bf16_f32 v186, v196, v197\n" \
  "v_exp_f32 v193, v161\n" \
  "v_exp_f32 v194, v162\n" \
  "v_cvt_pk_bf16_f32 v187, v198, v199\n" \
  "v_exp_f32 v195, v163\n" \
  "v_mfma_f32_32x32x16_bf16 %3, v[208:211], v[188:191], %3\n" \
  "v_exp_f32 v196, v164\n" \
  "v_cvt_pk_bf16_f32 v188, v192, v193\n" \
  "v_exp_f32 v197, v165\n" \
  "v_exp_f32 v198, v166\n" \
  "v_cvt_pk_bf16_f32 v189, v194, v195\n" \
  "v_exp_f32 v199, v167\n" \
  "v_cvt_pk_bf16_f32 v190, v196, v197\n" \
  "v_cvt_pk_bf16_f32 v191, v198, v199\n" \
  "s_waitcnt vmcnt(8)\n" \
  "v_mfma_f32_32x32x16_bf16 %0, v[216:219], v[184:187], %0\n" \
  "v_exp_f32 v192, v168\n" \
  "v_exp_f32 v193, v169\n" \
  "v_exp_f32 v194, v170\n" \
  "v_cvt_pk_bf16_f32 v184, v192, v193\n" \
  "v_exp_f32 v195, v171\n" \
  "v_mfma_f32_32x32x16_bf16 v[152:167], v[212:215], %6, 0\n" \
  "v_exp_f32 v196, v172\n" \
  "v_cvt_pk_bf16_f32 v185, v194, v195\n" \
  "v_exp_f32 v197, v173\n" \
  "v_exp_f32 v198, v174\n" \
  "v_exp_f32 v199, v175\n" \
  "v_mfma_f32_32x32x8bf16_1k v[152:167], v[250:251], %10, v[152:167]\n" \
  "v_exp_f32 v192, v176\n" \
  "v_cvt_pk_bf16_f32 v186, v196, v197\n" \
  "v_exp_f32 v193, v177\n" \
  "v_exp_f32 v194, v178\n" \
  "v_cvt_pk_bf16_f32 v187, v198, v199\n" \
  "v_exp_f32 v195, v179\n" \
  "v_mfma_f32_32x32x16_bf16 %0, v[220:223], v[188:191], %0\n" \
  "v_exp_f32 v196, v180\n" \
  "v_cvt_pk_bf16_f32 v188, v192, v193\n" \
  "v_exp_f32 v197, v181\n" \
  "v_exp_f32 v198, v182\n" \
  "v_cvt_pk_bf16_f32 v189, v194, v195\n" \
  "v_exp_f32 v199, v183\n" \
  "v_cvt_pk_bf16_f32 v190, v196, v197\n" \
  "v_cvt_pk_bf16_f32 v191, v198, v199\n" \
  "v_mfma_f32_32x32x16_bf16 %1, v[216:219], v[184:187], %1\n" \
  "v_exp_f32 v192, v152\n" \
  "v_exp_f32 v193, v153\n" \
  "v_exp_f32 v194, v154\n" \
  "v_cvt_pk_bf16_f32 v184, v192, v193\n" \
  "v_exp_f32 v195, v155\n" \
  "v_mfma_f32_32x32x16_bf16 v[168:183], v[212:215], %7, 0\n" \
  "v_exp_f32 v196, v156\n" \
  "v_cvt_pk_bf16_f32 v185, v194, v195\n" \
  "v_exp_f32 v197, v157\n" \
  "v_exp_f32 v198, v158\n" \
  "v_exp_f32 v199, v159\n" \
  "v_mfma_f32_32x32x8bf16_1k v[168:183], v[250:251], %11, v[168:183]\n" \
  "v_exp_f32 v192, v160\n" \
  "v_cvt_pk_bf16_f32 v186, v196, v197\n" \
  "v_exp_f32 v193, v161\n" \
  "v_exp_f32 v194, v162\n" \
  "v_cvt_pk_bf16_f32 v187, v198, v199\n" \
  "v_exp_f32 v195, v163\n" \
  "v_mfma_f32_32x32x16_bf16 %1, v[220:223], v[188:191], %1\n" \
  "v_exp_f32 v196, v164\n" \
  "v_cvt_pk_bf16_f32 v188, v192, v193\n" \
  "v_exp_f32 v197, v165\n" \
  "v_exp_f32 v198, v166\n" \
  "v_cvt_pk_bf16_f32 v189, v194, v195\n" \
  "v_exp_f32 v199, v167\n" \
  "v_cvt_pk_bf16_f32 v190, v196, v197\n" \
  "v_cvt_pk_bf16_f32 v191, v198, v199\n" \
  "v_mfma_f32_32x32x16_bf16 %2, v[216:219], v[184:187], %2\n" \
  "v_exp_f32 v192, v168\n" \
  "v_exp_f32 v193, v169\n" \
  "v_exp_f32 v194, v170\n" \
  "v_cvt_pk_bf16_f32 v184, v192, v193\n" \
  "v_exp_f32 v195, v171\n" \
  "s_waitcnt vmcnt(6)\n" \
  "v_mfma_f32_32x32x16_bf16 v[152:167], v[224:227], %4, 0\n" \
  "v_exp_f32 v196, v172\n" \
  "v_cvt_pk_bf16_f32 v185, v194, v195\n" \
  "v_exp_f32 v197, v173\n" \
  "v_exp_f32 v198, v174\n" \
  "v_exp_f32 v199, v175\n" \
  "v_mfma_f32_32x32x8bf16_1k v[152:167], v[252:253], %8, v[152:167]\n" \
  "v_exp_f32 v192, v176\n" \
  "v_cvt_pk_bf16_f32 v186, v196, v197\n" \
  "v_exp_f32 v193, v177\n" \
  "v_exp_f32 v194, v178\n" \
  "v_cvt_pk_bf16_f32 v187, v198, v199\n" \
  "v_exp_f32 v195, v179\n" \
  "v_mfma_f32_32x32x16_bf16 %2, v[220:223], v[188:191], %2\n" \
  "v_exp_f32 v196, v180\n" \
  "v_cvt_pk_bf16_f32 v188, v192, v193\n" \
  "v_exp_f32 v197, v181\n" \
  "v_exp_f32 v198, v182\n" \
  "v_cvt_pk_bf16_f32 v189, v194, v195\n" \
  "v_exp_f32 v199, v183\n" \
  "v_cvt_pk_bf16_f32 v190, v196, v197\n" \
  "v_cvt_pk_bf16_f32 v191, v198, v199\n" \
  "v_mfma_f32_32x32x16_bf16 %3, v[216:219], v[184:187], %3\n" \
  "v_exp_f32 v192, v152\n" \
  "v_exp_f32 v193, v153\n" \
  "v_exp_f32 v194, v154\n" \
  "v_cvt_pk_bf16_f32 v184, v192, v193\n" \
  "v_exp_f32 v195, v155\n" \
  "v_mfma_f32_32x32x16_bf16 v[168:183], v[224:227], %5, 0\n" \
  "v_exp_f32 v196, v156\n" \
  "v_cvt_pk_bf16_f32 v185, v194, v195\n" \
  "v_exp_f32 v197, v157\n" \
  "v_exp_f32 v198, v158\n" \
  "v_exp_f32 v199, v159\n" \
  "v_mfma_f32_32x32x8bf16_1k v[168:183], v[252:253], %9, v[168:183]\n" \
  "v_exp_f32 v192, v160\n" \
  "v_cvt_pk_bf16_f32 v186, v196, v197\n" \
  "v_exp_f32 v193, v161\n" \
  "v_exp_f32 v194, v162\n" \
  "v_cvt_pk_bf16_f32 v187, v198, v199\n" \
  "v_exp_f32 v195, v163\n" \
  "v_mfma_f32_32x32x16_bf16 %3, v[220:223], v[188:191], %3\n" \
  "v_exp_f32 v196, v164\n" \
  "v_cvt_pk_bf16_f32 v188, v192, v193\n" \
  "v_exp_f32 v197, v165\n" \
  "v_exp_f32 v198, v166\n" \
  "v_cvt_pk_bf16_f32 v189, v194, v195\n" \
  "v_exp_f32 v199, v167\n" \
  "v_cvt_pk_bf16_f32 v190, v196, v197\n" \
  "v_cvt_pk_bf16_f32 v191, v198, v199\n" \
  "s_waitcnt vmcnt(4)\n" \
  "v_mfma_f32_32x32x16_bf16 %0, v[228:231], v[184:187], %0\n" \
  "v_exp_f32 v192, v168\n" \
  "v_exp_f32 v193, v169\n" \
  "v_exp_f32 v194, v170\n" \
  "v_cvt_pk_bf16_f32 v184, v192, v193\n" \
  "v_exp_f32 v195, v171\n" \
  "v_mfma_f32_32x32x16_bf16 v[152:167], v[224:227], %6, 0\n" \
  "v_exp_f32 v196, v172\n" \
  "v_cvt_pk_bf16_f32 v185, v194, v195\n" \
  "v_exp_f32 v197, v173\n" \
  "v_exp_f32 v198, v174\n" \
  "v_exp_f32 v199, v175\n" \
  "v_mfma_f32_32x32x8bf16_1k v[152:167], v[252:253], %10, v[152:167]\n" \
  "v_exp_f32 v192, v176\n" \
  "v_cvt_pk_bf16_f32 v186, v196, v197\n" \
  "v_exp_f32 v193, v177\n" \
  "v_exp_f32 v194, v178\n" \
  "v_cvt_pk_bf16_f32 v187, v198, v199\n" \
  "v_exp_f32 v195, v179\n" \
  "v_mfma_f32_32x32x16_bf16 %0, v[232:235], v[188:191], %0\n" \
  "v_exp_f32 v196, v180\n" \
  "v_cvt_pk_bf16_f32 v188, v192, v193\n" \
  "v_exp_f32 v197, v181\n" \
  "v_exp_f32 v198, v182\n" \
  "v_cvt_pk_bf16_f32 v189, v194, v195\n" \
  "v_exp_f32 v199, v183\n" \
  "v_cvt_pk_bf16_f32 v190, v196, v197\n" \
  "v_cvt_pk_bf16_f32 v191, v198, v199\n" \
  "v_mfma_f32_32x32x16_bf16 %1, v[228:231], v[184:187], %1\n" \
  "v_exp_f32 v192, v152\n" \
  "v_exp_f32 v193, v153\n" \
  "v_exp_f32 v194, v154\n" \
  "v_cvt_pk_bf16_f32 v184, v192, v193\n" \
  "v_exp_f32 v195, v155\n" \
  "v_mfma_f32_32x32x16_bf16 v[168:183], v[224:227], %7, 0\n" \
  "v_exp_f32 v196, v156\n" \
  "v_cvt_pk_bf16_f32 v185, v194, v195\n" \
  "v_exp_f32 v197, v157\n" \
  "v_exp_f32 v198, v158\n" \
  "v_exp_f32 v199, v159\n" \
  "v_mfma_f32_32x32x8bf16_1k v[168:183], v[252:253], %11, v[168:183]\n" \
  "v_exp_f32 v192, v160\n" \
  "v_cvt_pk_bf16_f32 v186, v196, v197\n" \
  "v_exp_f32 v193, v161\n" \
  "v_exp_f32 v194, v162\n" \
  "v_cvt_pk_bf16_f32 v187, v198, v199\n" \
  "v_exp_f32 v195, v163\n" \
  "v_mfma_f32_32x32x16_bf16 %1, v[232:235], v[188:191], %1\n" \
  "v_exp_f32 v196, v164\n" \
  "v_cvt_pk_bf16_f32 v188, v192, v193\n" \
  "v_exp_f32 v197, v165\n" \
  "v_exp_f32 v198, v166\n" \
  "v_cvt_pk_bf16_f32 v189, v194, v195\n" \
  "v_exp_f32 v199, v167\n" \
  "v_cvt_pk_bf16_f32 v190, v196, v197\n" \
  "v_cvt_pk_bf16_f32 v191, v198, v199\n" \
  "v_mfma_f32_32x32x16_bf16 %2, v[228:231], v[184:187], %2\n" \
  "v_exp_f32 v192, v168\n" \
  "v_exp_f32 v193, v169\n" \
  "v_exp_f32 v194, v170\n" \
  "v_cvt_pk_bf16_f32 v184, v192, v193\n" \
  "v_exp_f32 v195, v171\n" \
  "s_waitcnt vmcnt(2)\n" \
  "v_mfma_f32_32x32x16_bf16 v[152:167], v[236:239], %4, 0\n" \
  "v_exp_f32 v196, v172\n" \
  "v_cvt_pk_bf16_f32 v185, v194, v195\n" \
  "v_exp_f32 v197, v173\n" \
  "v_exp_f32 v198, v174\n" \
  "v_exp_f32 v199, v175\n" \
  "v_mfma_f32_32x32x8bf16_1k v[152:167], v[254:255], %8, v[152:167]\n" \
  "v_exp_f32 v192, v176\n" \
  "v_cvt_pk_bf16_f32 v186, v196, v197\n" \
  "v_exp_f32 v193, v177\n" \
  "v_exp_f32 v194, v178\n" \
  "v_cvt_pk_bf16_f32 v187, v198, v199\n" \
  "v_exp_f32 v195, v179\n" \
  "v_mfma_f32_32x32x16_bf16 %2, v[232:235], v[188:191], %2\n" \
  "v_exp_f32 v196, v180\n" \
  "v_cvt_pk_bf16_f32 v188, v192, v193\n" \
  "v_exp_f32 v197, v181\n" \
  "v_exp_f32 v198, v182\n" \
  "v_cvt_pk_bf16_f32 v189, v194, v195\n" \
  "v_exp_f32 v199, v183\n" \
  "v_cvt_pk_bf16_f32 v190, v196, v197\n" \
  "v_cvt_pk_bf16_f32 v191, v198, v199\n" \
  "v_mfma_f32_32x32x16_bf16 %3, v[228:231], v[184:187], %3\n" \
  "v_exp_f32 v192, v152\n" \
  "v_exp_f32 v193, v153\n" \
  "v_exp_f32 v194, v154\n" \
  "v_cvt_pk_bf16_f32 v184, v192, v193\n" \
  "v_exp_f32 v195, v155\n" \
  "v_mfma_f32_32x32x16_bf16 v[168:183], v[236:239], %5, 0\n" \
  "v_exp_f32 v196, v156\n" \
  "v_cvt_pk_bf16_f32 v185, v194, v195\n" \
  "v_exp_f32 v197, v157\n" \
  "v_exp_f32 v198, v158\n" \
  "v_exp_f32 v199, v159\n" \
  "v_mfma_f32_32x32x8bf16_1k v[168:183], v[254:255], %9, v[168:183]\n" \
  "v_exp_f32 v192, v160\n" \
  "v_cvt_pk_bf16_f32 v186, v196, v197\n" \
  "v_exp_f32 v193, v161\n" \
  "v_exp_f32 v194, v162\n" \
  "v_cvt_pk_bf16_f32 v187, v198, v199\n" \
  "v_exp_f32 v195, v163\n" \
  "v_mfma_f32_32x32x16_bf16 %3, v[232:235], v[188:191], %3\n" \
  "v_exp_f32 v196, v164\n" \
  "v_cvt_pk_bf16_f32 v188, v192, v193\n" \
  "v_exp_f32 v197, v165\n" \
  "v_exp_f32 v198, v166\n" \
  "v_cvt_pk_bf16_f32 v189, v194, v195\n" \
  "v_exp_f32 v199, v167\n" \
  "v_cvt_pk_bf16_f32 v190, v196, v197\n" \
  "v_cvt_pk_bf16_f32 v191, v198, v199\n" \
  "s_waitcnt vmcnt(0)\n" \
  "v_mfma_f32_32x32x16_bf16 %0, v[240:243], v[184:187], %0\n" \
  "v_exp_f32 v192, v168\n" \
  "v_exp_f32 v193, v169\n" \
  "v_exp_f32 v194, v170\n" \
  "v_cvt_pk_bf16_f32 v184, v192, v193\n" \
  "v_exp_f32 v195, v171\n" \
  "v_mfma_f32_32x32x16_bf16 v[152:167], v[236:239], %6, 0\n" \
  "v_exp_f32 v196, v172\n" \
  "v_cvt_pk_bf16_f32 v185, v194, v195\n" \
  "v_exp_f32 v197, v173\n" \
  "v_exp_f32 v198, v174\n" \
  "v_exp_f32 v199, v175\n" \
  "v_mfma_f32_32x32x8bf16_1k v[152:167], v[254:255], %10, v[152:167]\n" \
  "v_exp_f32 v192, v176\n" \
  "v_cvt_pk_bf16_f32 v186, v196, v197\n" \
  "v_exp_f32 v193, v177\n" \
  "v_exp_f32 v194, v178\n" \
  "v_cvt_pk_bf16_f32 v187, v198, v199\n" \
  "v_exp_f32 v195, v179\n" \
  "v_mfma_f32_32x32x16_bf16 %0, v[244:247], v[188:191], %0\n" \
  "v_exp_f32 v196, v180\n" \
  "v_cvt_pk_bf16_f32 v188, v192, v193\n" \
  "v_exp_f32 v197, v181\n" \
  "v_exp_f32 v198, v182\n" \
  "v_cvt_pk_bf16_f32 v189, v194, v195\n" \
  "v_exp_f32 v199, v183\n" \
  "v_cvt_pk_bf16_f32 v190, v196, v197\n" \
  "v_cvt_pk_bf16_f32 v191, v198, v199\n" \
  "v_mfma_f32_32x32x16_bf16 %1, v[240:243], v[184:187], %1\n" \
  "v_exp_f32 v192, v152\n" \
  "v_exp_f32 v193, v153\n" \
  "v_exp_f32 v194, v154\n" \
  "v_cvt_pk_bf16_f32 v184, v192, v193\n" \
  "v_exp_f32 v195, v155\n" \
  "v_mfma_f32_32x32x16_bf16 v[168:183], v[236:239], %7, 0\n" \
  "v_exp_f32 v196, v156\n" \
  "v_cvt_pk_bf16_f32 v185, v194, v195\n" \
  "v_exp_f32 v197, v157\n" \
  "v_exp_f32 v198, v158\n" \
  "v_exp_f32 v199, v159\n" \
  "v_mfma_f32_32x32x8bf16_1k v[168:183], v[254:255], %11, v[168:183]\n" \
  "v_exp_f32 v192, v160\n" \
  "v_cvt_pk_bf16_f32 v186, v196, v197\n" \
  "v_exp_f32 v193, v161\n" \
  "v_exp_f32 v194, v162\n" \
  "v_cvt_pk_bf16_f32 v187, v198, v199\n" \
  "v_exp_f32 v195, v163\n" \
  "v_mfma_f32_32x32x16_bf16 %1, v[244:247], v[188:191], %1\n" \
  "v_exp_f32 v196, v164\n" \
  "v_cvt_pk_bf16_f32 v188, v192, v193\n" \
  "v_exp_f32 v197, v165\n" \
  "v_exp_f32 v198, v166\n" \
  "v_cvt_pk_bf16_f32 v189, v194, v195\n" \
  "v_exp_f32 v199, v167\n" \
  "v_cvt_pk_bf16_f32 v190, v196, v197\n" \
  "v_cvt_pk_bf16_f32 v191, v198, v199\n" \
  "v_mfma_f32_32x32x16_bf16 %2, v[240:243], v[184:187], %2\n" \
  "v_exp_f32 v192, v168\n" \
  "v_exp_f32 v193, v169\n" \
  "v_exp_f32 v194, v170\n" \
  "v_cvt_pk_bf16_f32 v184, v192, v193\n" \
  "v_exp_f32 v195, v171\n" \
  "s_nop 0\n" \
  "v_exp_f32 v196, v172\n" \
  "v_cvt_pk_bf16_f32 v185, v194, v195\n" \
  "v_exp_f32 v197, v173\n" \
  "v_exp_f32 v198, v174\n" \
  "v_exp_f32 v199, v175\n" \
  "s_nop 0\n" \
  "v_exp_f32 v192, v176\n" \
  "v_cvt_pk_bf16_f32 v186, v196, v197\n" \
  "v_exp_f32 v193, v177\n" \
  "v_exp_f32 v194, v178\n" \
  "v_cvt_pk_bf16_f32 v187, v198, v199\n" \
  "v_exp_f32 v195, v179\n" \
  "v_mfma_f32_32x32x16_bf16 %2, v[244:247], v[188:191], %2\n" \
  "v_exp_f32 v196, v180\n" \
  "v_cvt_pk_bf16_f32 v188, v192, v193\n" \
  "v_exp_f32 v197, v181\n" \
  "v_exp_f32 v198, v182\n" \
  "v_cvt_pk_bf16_f32 v189, v194, v195\n" \
  "v_exp_f32 v199, v183\n" \
  "v_cvt_pk_bf16_f32 v190, v196, v197\n" \
  "v_cvt_pk_bf16_f32 v191, v198, v199\n" \
  "s_nop 1\n" \
  "v_mfma_f32_32x32x16_bf16 %3, v[240:243], v[184:187], %3\n" \
  "v_mfma_f32_32x32x16_bf16 %3, v[244:247], v[188:191], %3\n" \
  "s_setprio 0\n" \
  "s_nop 15\n"
#define ATT_CLOB "s44", "v152", "v153", "v154", "v155", "v156", "v157", "v158", "v159", "v160", "v161", "v162", "v163", "v164", "v165", "v166", "v167", "v168", "v169", "v170", "v171", "v172", "v173", "v174", "v175", "v176", "v177", "v178", "v179", "v180", "v181", "v182", "v183", "v184", "v185", "v186", "v187", "v188", "v189", "v190", "v191", "v192", "v193", "v194", "v195", "v196", "v197", "v198", "v199", "v200", "v201", "v202", "v203", "v204", "v205", "v206", "v207", "v208", "v209", "v210", "v211", "v212", "v213", "v214", "v215", "v216", "v217", "v218", "v219", "v220", "v221", "v222", "v223", "v224", "v225", "v226", "v227", "v228", "v229", "v230", "v231", "v232", "v233", "v234", "v235", "v236", "v237", "v238", "v239", "v240", "v241", "v242", "v243", "v244", "v245", "v246", "v247", "v248", "v249", "v250", "v251", "v252", "v253", "v254", "v255", "s40", "s41", "s42", "scc", "memory"


DEVI f32x16 mfma16(u32x4 a, u32x4 b, f32x16 c) { return __builtin_amdgcn_mfma_f32_32x32x16_bf16(__builtin_bit_cast(bf16x8, a), __builtin_bit_cast(bf16x8, b), c, 0, 0, 0); }
DEVI f32x16 mfma8(u32x2 a, u32x2 b, f32x16 c) { return __builtin_amdgcn_mfma_f32_32x32x8bf16_1k(__builtin_bit_cast(s16x4, a), __builtin_bit_cast(s16x4, b), c, 0, 0, 0); }
DEVI unsigned pk(float lo, float hi) { f32x2 v = {lo, hi}; return __builtin_bit_cast(unsigned, __builtin_convertvector(v, bf16x2)); }
DEVI float dot2(unsigned a, unsigned b, float c) { return __builtin_amdgcn_fdot2_f32_bf16(__builtin_bit_cast(bf16x2, a), __builtin_bit_cast(bf16x2, b), c, false); }
DEVI float bf_lo(unsigned u) { return __uint_as_float(u << 16); }
DEVI float bf_hi(unsigned u) { return __uint_as_float(u & 0xffff0000u); }
DEVI u32x4 lds128(lptr p) { return *(LAS u32x4*)p; }
DEVI u32x2 lds64(lptr p) { return *(LAS u32x2*)p; }
DEVI u32x2 ldstr(lptr p) { return __builtin_bit_cast(u32x2, __builtin_amdgcn_ds_read_tr16_b64_v4i16((LAS s16x4*)p)); }
DEVI u32x4 ldstr2(lptr p, int d) { u32x2 a = ldstr(p), b = ldstr(p + d); return (u32x4){a[0], a[1], b[0], b[1]}; }
DEVI void glds16(const void* gbase, unsigned voff, unsigned lds_addr) {
  unsigned sv;
  asm volatile("s_mov_b32 %0, m0\n\ts_mov_b32 m0, %3\n\ts_nop 0\n\tglobal_load_lds_dwordx4 %1, %2\n\ts_mov_b32 m0, %0"
               : "=&s"(sv) : "v"(voff), "s"(gbase), "s"(lds_addr) : "memory");
}
#define WAIT_VM(n) asm volatile("s_waitcnt vmcnt(" #n ")" ::: "memory")
#define WAIT_LGKM0() asm volatile("s_waitcnt lgkmcnt(0)" ::: "memory")
#define BAR() asm volatile("s_waitcnt lgkmcnt(0)\n\ts_barrier" ::: "memory")
DEVI float xhalf(float v, int h) { auto r = __builtin_amdgcn_permlane32_swap(__float_as_uint(v), __float_as_uint(v), false, false); return __uint_as_float(h ? r[0] : r[1]); }
DEVI void lds_arrive(unsigned addr) {
  unsigned long long sv;
  asm volatile("s_mov_b64 %0, exec\n\ts_mov_b64 exec, 1\n\ts_waitcnt lgkmcnt(0)\n\tds_add_u32 %1, %2\n\ts_mov_b64 exec, %0"
               : "=&s"(sv) : "v"(addr), "v"(1u) : "memory");
}
DEVI bool lds_arrive_last(unsigned addr, unsigned before) {
  unsigned long long sv; unsigned o = 1u, res;
  asm volatile("s_mov_b64 %0, exec\n\ts_mov_b64 exec, 1\n\ts_waitcnt lgkmcnt(0)\n\tds_add_rtn_u32 %1, %3, %1\n\ts_waitcnt lgkmcnt(0)\n\t"
               "v_readfirstlane_b32 %2, %1\n\ts_mov_b64 exec, %0\n\ts_nop 0"
               : "=&s"(sv), "+v"(o), "=s"(res) : "v"(addr) : "memory");
  return res == before;
}
template <int SLEEP> DEVI void lds_wait_t(unsigned addr, unsigned target) {
  unsigned v = 0u, t, n;
  asm volatile("s_mov_b32 %2, 0x40000\n"
               "1:\n\tds_read_b32 %0, %3\n\ts_waitcnt lgkmcnt(0)\n\tv_readfirstlane_b32 %1, %0\n\ts_cmp_ge_u32 %1, %4\n\ts_cbranch_scc1 2f\n\t"
               "s_sleep %c5\n\ts_sub_u32 %2, %2, 1\n\ts_cbranch_scc0 1b\n2:"
               : "+&v"(v), "=&s"(t), "=&s"(n) : "v"(addr), "s"(target), "i"(SLEEP) : "scc", "memory");
}
#define lds_wait lds_wait_t<1>
#define lds_wait_mid lds_wait_t<3>
#define lds_wait_slow lds_wait_t<10>
DEVI void glb_wait(gu32* f, unsigned tag) {
#pragma unroll 1
  for (unsigned spins = 0; spins < SPIN_CAP; ++spins) {
    if (__all(__hip_atomic_load(f, RLX_AGENT) == tag)) break;
    __builtin_amdgcn_s_sleep(16);
  }
  __builtin_amdgcn_fence(__ATOMIC_ACQUIRE, "wavefront");
  asm volatile("" ::: "memory");
}
DEVI void block_coords(int bid, int& rb, int& hg) { const int xcd = bid & 7, idx = bid >> 3; rb = (xcd >> 1) * 8 + (idx >> 2); hg = (xcd & 1) * 4 + (idx & 3); }
DEVI unsigned x_src_off(int row, int lane) { return row * 1280 + (((lane & 15) ^ (row & 15)) << 4); }
DEVI unsigned x_frag_off(int r, int h) { return r * 256 + (((h ^ r) & 15) << 4); }

#define CODE_TOUCH(off, ldsdst) do { unsigned sv_, o_ = (off); \
    asm volatile("v_min_u32 %1, k_main_end-k_main, %1\n\ts_getpc_b64 s[44:45]\n\ts_add_u32 s44, s44, k_main@rel32@lo+4\n\ts_addc_u32 s45, s45, k_main@rel32@hi+12\n\t" \
                 "s_mov_b32 %0, m0\n\ts_mov_b32 m0, %2\n\ts_nop 0\n\tglobal_load_lds_dword %1, s[44:45]\n\ts_mov_b32 m0, %0" \
                 : "=&s"(sv_), "+v"(o_) : "s"(ldsdst) : "s44", "s45", "scc", "memory"); } while (0)
DEVI void touch_line(const void* p, unsigned lds_addr) {
  unsigned sv;
  asm volatile("s_mov_b32 %0, m0\n\ts_mov_b32 m0, %2\n\ts_nop 0\n\tglobal_load_lds_dword %1, off\n\ts_mov_b32 m0, %0" : "=&s"(sv) : "v"(p), "s"(lds_addr) : "memory");
}
DEVI void touch_line_sc1(const void* p, unsigned lds_addr) {
  unsigned sv;
  asm volatile("s_mov_b32 %0, m0\n\ts_mov_b32 m0, %2\n\ts_nop 0\n\tglobal_load_lds_dword %1, off sc1\n\ts_mov_b32 m0, %0" : "=&s"(sv) : "v"(p), "s"(lds_addr) : "memory");
}
DEVI float gate_of(f32x4 p) { return __builtin_amdgcn_rcpf(1.f + __expf(-((p[0] + p[1]) + (p[2] + p[3])))); }
DEVI void st128(__amdgpu_buffer_rsrc_t rs, u32x4 v, unsigned vo, unsigned so) { __builtin_amdgcn_raw_buffer_store_b128(v, rs, vo + so, 0, AUX_SC1); }
DEVI unsigned umax(unsigned a, unsigned b) { return a > b ? a : b; }
DEVI float wave_max(float v) {
#define DPP_MAX(c) v = fmaxf(v, __uint_as_float(__builtin_amdgcn_update_dpp(0u, __float_as_uint(v), c, 0xf, 0xf, true)))
  DPP_MAX(0x121); DPP_MAX(0x122); DPP_MAX(0x124); DPP_MAX(0x128);
  auto a = __builtin_amdgcn_permlane16_swap(__float_as_uint(v), __float_as_uint(v), false, false);
  v = fmaxf(__uint_as_float(a[0]), __uint_as_float(a[1]));
  auto b = __builtin_amdgcn_permlane32_swap(__float_as_uint(v), __float_as_uint(v), false, false);
  return fmaxf(__uint_as_float(b[0]), __uint_as_float(b[1]));
}

constexpr int M_X = 0, M_W = 81920, M_SLOT = 20480, M_KVP = 148480;
constexpr int M_QX = M_KVP;
constexpr int M_WO = 0, M_IMG = 102400, M_RED = 102400;
constexpr int M_SYNC = 160768, M_GATE = M_SYNC + 256, M_DUMP = M_GATE + 1024, M_LDS = M_DUMP + 256 + 16;
constexpr int IMG_P = 488;
enum { SY_PK = 0, SY_PR = 16, SY_F0 = 32, SY_F1 = 20, SY_GO = 44, SY_R1 = 80, SY_R2 = 84, SY_R3 = 88, SY_R4 = 92, SY_Q = 24, SY_KV = 28, SY_KI = 40, SY_KP = 56, SY_KS = 60, SY_K4 = 36, SY_AD = 72, SY_HP = 48, SY_HR = 52, SY_BR = 68, SY_OB = 64, SY_G = 76, SY_B = 96, SY_HD = 112, SY_P0 = 128, SY_P1 = 144, SY_KK = 160 };
#define SB() __builtin_amdgcn_sched_barrier(0)
#define QUADS(qd, b0, b1, b2) do { _Pragma("unroll") for (int q = 0; q < 4; ++q) { qd[q] = (f32x4){b0[4 * q], b0[4 * q + 1], b0[4 * q + 2], b0[4 * q + 3]}; \
    qd[4 + q] = (f32x4){b1[4 * q], b1[4 * q + 1], b1[4 * q + 2], b1[4 * q + 3]}; } \
    qd[8] = (f32x4){b2[0], b2[1], b2[2], b2[3]}; qd[9] = (f32x4){b2[4], b2[5], b2[6], b2[7]}; } while (0)
typedef GAS unsigned long long gu64;
#define BLD128(vo, so, aux) __builtin_amdgcn_raw_buffer_load_b128(rs, vo, so, aux)

DEVI unsigned pack_off(int i, int lane, int pitch, int rh, int rt) {
  const unsigned P = i * 64 + lane, rho = (P * 3277u) >> 16;
  return ((rho >> 3) * rh + ((rho >> 2) & 1) * rt + (rho & 3)) * pitch + (P - rho * 20) * 16;
}

extern "C" __global__ void __launch_bounds__(512, 2) k_main(const char* __restrict__ x, const char* __restrict__ wq, const char* __restrict__ wk,
                                                         const char* __restrict__ wv, const char* __restrict__ woA, const char* __restrict__ wqkv,
                                                         const char* __restrict__ woB, const char* __restrict__ gw, char* __restrict__ ws, char* __restrict__ out) {
  extern __shared__ __attribute__((aligned(16))) char smem[];
  const lptr L = (lptr)smem;
  const unsigned L0 = (unsigned)(uintptr_t)L;
  const int tid = threadIdx.x, lane = tid & 63, w = __builtin_amdgcn_readfirstlane(tid >> 6), r = lane & 31, h = lane >> 5;
  CODE_TOUCH(((blockIdx.x >> 3) + 32 * lane) * 128u, L0 + M_DUMP);
  asm volatile("" :: "s"(x), "s"(wq), "s"(wk), "s"(wv), "s"(woA), "s"(wqkv), "s"(woB), "s"(gw), "s"(ws), "s"(out));
  int rb, hg; block_coords(blockIdx.x, rb, hg);
  const int blk_id = rb * 8 + hg, bidx = blockIdx.x >> 3;
  const bool extra = !(hg & 1), isV = (hg >> 1) & 1; const int mte = hg >> 2;
  const int q4 = (lane & 15) >> 2, p4 = lane & 3, blk = (lane >> 4) & 1;
  const unsigned xf = x_frag_off(r, h), l16 = lane * 16;
  const __amdgpu_buffer_rsrc_t rs = __builtin_amdgcn_make_buffer_rsrc((void*)ws, 0, (int)WS_END, 0x00020000);
  gu64* const gran = (gu64*)(ws + WS_GRAN);
  const char* xb = x + rb * 81920;
  unsigned tagv = 0u, tag;
  asm volatile("global_load_dword %0, %0, %1 sc1" : "+v"(tagv) : "s"(ws + WS_EPOCH) : "memory");
#define MAKE_TAG() do { asm volatile("" : "+v"(tagv)); const unsigned p_ = __builtin_amdgcn_readfirstlane(tagv), t_ = (p_ ^ 0x9E3779B9u) * 0x85EBCA6Bu + 0x632BE5ABu; tag = t_ == p_ ? t_ + 1u : t_; } while (0)
  const int w4 = w & 3;
  const char* wqb = wq + hg * 160;
#define W_SRC(P) ((((P) / 10 & ~7u) | (((P) / 10 & 1u) << 2) | (((P) / 10 >> 1) & 3u)) * 1280 + ((P) % 10) * 16)
  {
    const unsigned xo0 = x_src_off(8 * w4 + (lane >> 4), lane), xo1 = x_src_off(8 * w4 + 4 + (lane >> 4), lane);
    const int mh = w < 4 ? mte ^ 1 : mte;
    if (w < 4) {
      BAR();
    }
#pragma unroll
    for (int ch = 0; ch < 5; ++ch) {
      glds16(xb + mh * 40960 + ch * 256, xo0, L0 + M_X + ch * 16384 + mh * 8192 + w4 * 2048);
      glds16(xb + mh * 40960 + ch * 256, xo1, L0 + M_X + ch * 16384 + mh * 8192 + w4 * 2048 + 1024);
    }
    if (w >= 4) {
      const int job = rb < 30 ? rb * 4 + w4 : w4;
      const char* src = extra ? (isV ? wv : wk) + w4 * 5120 : wqkv + (job / 40) * 1280 + (job % 40) * 61440 + (hg - 1) * 160;
#pragma unroll
      for (int i = 0; i < 7; ++i) {
        const unsigned po = pack_off(i < 5 ? i : 0, lane, 3840, 8, 4), vo = extra ? i * 1024 + (i < 5 ? 0 : 15360 - w4 * 3840) + l16 : po;
        if (i < 6 || lane < 16) glds16(src, vo, L0 + M_W + 2 * M_SLOT + w4 * 6400 + i * 1024);
      }
      if (w == 7) *(LAS unsigned*)(L + M_SYNC + lane * 4) = 0u;
      BAR();
    }
  }
  f32x16 b0, b1, b2;

  if (w < 4) {
    const int rg = w & 1, kg = w >> 1;
    __builtin_amdgcn_s_setprio(3);
    u32x4 qa[4]; u32x2 qb[4];
    {
      unsigned wo[5];
#pragma unroll
      for (int i = 0; i < 5; ++i) { const unsigned P = i * 64 + lane; wo[i] = W_SRC(P); }
#define W_ISSUE(ch) _Pragma("unroll") for (int i = 0; i < 5; ++i) glds16(wqb + (ch) * 163840 + w * 40960, wo[i], L0 + M_W + ((ch) % 3) * M_SLOT + w * 5120 + i * 1024)
      W_ISSUE(0);
      if (lane < 35) {
        const int l3 = (bidx >> 2) * 128 + w * 32 + lane;
        touch_line(lane < 32 ? wqb + (128 + (l3 >> 1)) * 1280 + (l3 & 1) * 128 : gw + w * 320 + (lane - 32) * 128, L0 + M_DUMP);
      }
      if (!extra) { W_ISSUE(1); }
      const unsigned wf = (8 * h + 2 * q4) * 160 + (40 * kg + 16 * blk + 4 * p4) * 2;
      f32x16 ac0 = {}, ac1 = {};
      u32x4 a_[2][8], c_[2][8], b_[2][8];
#define P_READ3(ch, st) do { const lptr xs_ = L + M_X + (ch) * 16384 + rg * 8192, ws_ = L + M_W + ((ch) % 3) * M_SLOT + wf; \
        _Pragma("unroll") for (int ks = 0; ks < 8; ++ks) { b_[st][ks] = lds128(xs_ + (xf ^ (ks << 5))); a_[st][ks] = ldstr2(ws_ + ks * 2560, 160); c_[st][ks] = ldstr2(ws_ + ks * 2560 + 40, 160); } SB(); } while (0)
#define P_READ(ch) P_READ3(ch, (ch) & 1)
#define P_MMA(ch, k0, k1) do { _Pragma("unroll") for (int ks = k0; ks < k1; ++ks) { ac0 = mfma16(a_[(ch) & 1][ks], b_[(ch) & 1][ks], ac0); ac1 = mfma16(c_[(ch) & 1][ks], b_[(ch) & 1][ks], ac1); } SB(); } while (0)
      if (extra) WAIT_VM(1); else WAIT_VM(6);
      asm volatile("" :: "v"(tagv));
      lds_arrive(L0 + M_SYNC + SY_KI); lds_wait(L0 + M_SYNC + SY_KI, 8);
#define P_STEP(chn, st, DM) do { const lptr xs_ = L + M_X + (chn) * 16384 + rg * 8192, ws_ = L + M_W + ((chn) == 2 ? 2 : (chn) - 3) * M_SLOT + wf; \
        _Pragma("unroll") for (int ks = 0; ks < 8; ++ks) { \
          ac0 = mfma16(a_[st][ks], b_[st][ks], ac0); SB(); \
          b_[(st) ^ 1][ks] = lds128(xs_ + (xf ^ (ks << 5))); a_[(st) ^ 1][ks] = ldstr2(ws_ + ks * 2560, 160); SB(); \
          ac1 = mfma16(c_[st][ks], b_[st][ks], ac1); SB(); \
          c_[(st) ^ 1][ks] = ldstr2(ws_ + ks * 2560 + 40, 160); \
          if ((DM) && ks < 5) glds16(wqb + (DM) * 163840 + w * 40960, wo[ks < 5 ? ks : 0], L0 + M_W + ((DM) % 3) * M_SLOT + w * 5120 + ks * 1024); \
          SB(); } } while (0)
      P_READ(0);
      lds_arrive(L0 + M_SYNC + SY_F0);
      P_MMA(0, 0, 4);
      lds_wait(L0 + M_SYNC + SY_F0, 4);
      if (extra) { lds_wait(L0 + M_SYNC + SY_KS, 1); W_ISSUE(1); }
      W_ISSUE(3);
      P_MMA(0, 4, 8);
      WAIT_VM(5); lds_arrive(L0 + M_SYNC + SY_R1);
      if (extra) { W_ISSUE(2); }
      lds_wait(L0 + M_SYNC + SY_R1, 4);
      P_READ(1);
      if (extra) WAIT_VM(5); else WAIT_VM(0);
      lds_arrive(L0 + M_SYNC + SY_R3); lds_wait(L0 + M_SYNC + SY_R3, 4);
      P_STEP(3, 1, 4);
      if (!extra) { lds_wait(L0 + M_SYNC + SY_KS, 1); W_ISSUE(2); }
      WAIT_VM(5); lds_arrive(L0 + M_SYNC + SY_R2); lds_wait(L0 + M_SYNC + SY_R2, 4);
      const int chA = extra ? 2 : 4;
      P_STEP(chA, 0, 0);
      WAIT_VM(0); lds_arrive(L0 + M_SYNC + SY_R4); lds_wait(L0 + M_SYNC + SY_R4, 4);
      P_STEP(6 - chA, 1, 0);
      P_MMA(0, 0, 4);
      lds_arrive(L0 + M_SYNC + SY_Q);
      P_MMA(0, 4, 8);
#pragma unroll
      for (int i = 0; i < 4; ++i) { qa[0][i] = pk(ac0[2 * i] * QSCALE, ac0[2 * i + 1] * QSCALE); qa[1][i] = pk(ac1[2 * i] * QSCALE, ac1[2 * i + 1] * QSCALE); }
#pragma unroll
      for (int i = 0; i < 2; ++i) { qb[0][i] = pk(ac0[8 + 2 * i] * QSCALE, ac0[9 + 2 * i] * QSCALE); qb[1][i] = pk(ac1[8 + 2 * i] * QSCALE, ac1[9 + 2 * i] * QSCALE); }
    }
    float m[4];
#define QNORM(c) do { float s_ = dot2(qa[c][0], qa[c][0], dot2(qa[c][1], qa[c][1], dot2(qa[c][2], qa[c][2], dot2(qa[c][3], qa[c][3], 0.f)))); \
      if (!h) s_ = dot2(qb[c][0], qb[c][0], dot2(qb[c][1], qb[c][1], s_)); m[c] = s_ + xhalf(s_, h); } while (0)
    QNORM(0); QNORM(1);
    *(LAS u32x4*)(L + M_QX + w * 3072 + l16) = qa[0];
    *(LAS u32x4*)(L + M_QX + w * 3072 + 1024 + l16) = qa[1];
    *(LAS u32x4*)(L + M_QX + w * 3072 + 2048 + l16) = h ? (u32x4){__float_as_uint(m[0]), 0u, __float_as_uint(m[1]), 0u} : (u32x4){qb[0][0], qb[0][1], qb[1][0], qb[1][1]};
    const unsigned sy_pair = L0 + M_SYNC + (rg ? SY_P1 : SY_P0);
    lds_arrive(sy_pair); lds_wait(sy_pair, 2);
    lds_wait(L0 + M_SYNC + SY_KV, 1);
    {
      qa[2] = lds128(L + M_QX + (w ^ 2) * 3072 + l16); qa[3] = lds128(L + M_QX + (w ^ 2) * 3072 + 1024 + l16);
      const u32x4 t = lds128(L + M_QX + (w ^ 2) * 3072 + 2048 + l16);
      qb[2] = (u32x2){t[0], t[1]}; qb[3] = (u32x2){t[2], t[3]};
      m[2] = __uint_as_float(t[0]); m[3] = __uint_as_float(t[2]);
      const float kk = *(LAS float*)(L + M_SYNC + SY_KK);
#pragma unroll
      for (int c = 0; c < 4; ++c) m[c] = bf_hi(__float_as_uint(__builtin_amdgcn_sqrtf(m[c] * kk) * 1.01f));
    }
    const char* kv = ws + WS_KV + kg * 32 * TILE_B;
    unsigned lq;
    const unsigned oKa = lane * 16, oKb = 1024 + lane * 8, oV = 1536 + h * 512 + (r < 21 ? r : 21) * 16;
    float fin[2][12], linv[2];
    unsigned pair_cnt = 2;
    int pass = 0;
    asm volatile("" : "+s"(pass));
#pragma unroll 1
    for (;; ++pass) {
#pragma unroll
      for (int c = 0; c < 4; ++c) {
        const float nm = -m[c], hi = bf_hi(__float_as_uint(nm)), r1 = nm - hi, mid = bf_hi(__float_as_uint(r1)), lo = r1 - mid;
        if (oKa >= 512u) qb[c] = (u32x2){(__float_as_uint(hi) >> 16) | __float_as_uint(mid), __float_as_uint(lo) >> 16};
      }
      f32x16 o0 = {}, o1 = {}, o2 = {}, o3 = {};
      lds_arrive(L0 + M_SYNC + SY_GO);
      __builtin_amdgcn_s_setprio(0);
      asm volatile(ATT_ASM
                   : "+v"(o0), "+v"(o1), "+v"(o2), "+v"(o3)
                   : "v"(qa[0]), "v"(qa[1]), "v"(qa[2]), "v"(qa[3]), "v"(qb[0]), "v"(qb[1]), "v"(qb[2]), "v"(qb[3]), "v"(oKa), "v"(oKb), "v"(oV),
                     "s"((unsigned)(uintptr_t)kv), "s"((unsigned)((uintptr_t)kv >> 32))
                   : ATT_CLOB);
      lq = oKa; asm volatile("" : "+v"(lq));
      const int h = lq >> 9, r = (lq >> 4) & 31;
      const lptr mine = L + M_RED + w * 7168 + lq, theirs = L + M_RED + (w ^ 2) * 7168 + lq;
      __builtin_amdgcn_s_setprio(3);
      lds_arrive(L0 + M_SYNC + SY_AD);
      lds_wait(L0 + M_SYNC + SY_HD, 4);
#pragma unroll
      for (int q = 0; q < 3; ++q) {
        *(LAS f32x4*)(mine + q * 1024) = (f32x4){o2[4 * q], o2[4 * q + 1], o2[4 * q + 2], o2[4 * q + 3]};
        *(LAS f32x4*)(mine + (3 + q) * 1024) = (f32x4){o3[4 * q], o3[4 * q + 1], o3[4 * q + 2], o3[4 * q + 3]};
      }
      *(LAS f32x2*)(mine + 6144) = (f32x2){o0[8], o1[8]};
      const float g = gate_of(*(LAS f32x4*)(L + M_GATE + (rg * 32 + r) * 16));
      pair_cnt += 2; lds_arrive(sy_pair); lds_wait(sy_pair, pair_cnt);
      const f32x2 lo_ = *(LAS f32x2*)(theirs + 6144);
#pragma unroll
      for (int q = 2; q >= 0; --q) {
        const f32x4 a = *(LAS f32x4*)(theirs + q * 1024), b = *(LAS f32x4*)(theirs + (3 + q) * 1024);
#pragma unroll
        for (int j = 0; j < 4; ++j) { fin[0][4 * q + j] = o0[4 * q + j] + a[j]; fin[1][4 * q + j] = o1[4 * q + j] + b[j]; }
      }
      SB();
      const float THR = 8.2718061e-25f;
      const bool ok = fin[0][8] >= THR && fin[1][8] >= THR && o2[8] + lo_[0] >= THR && o3[8] + lo_[1] >= THR;
#pragma unroll
      for (int t = 0; t < 2; ++t) { const float o = xhalf(fin[t][8], h); linv[t] = g * __builtin_amdgcn_rcpf(h ? fin[t][8] : o); }
      if (__builtin_expect(pass != 0 || __all(ok || !h), 1)) break;
      float mx[4] = {-INFINITY, -INFINITY, -INFINITY, -INFINITY};
#pragma unroll 1
      for (int t = 0; t < 32; ++t) {
        const u32x4 ka = BLD128(oKa, WS_KV + (kg * 32 + t) * TILE_B, AUX_SC1);
        const u32x2 kb = __builtin_amdgcn_raw_buffer_load_b64(rs, oKb, WS_KV + (kg * 32 + t) * TILE_B, AUX_SC1);
#pragma unroll
        for (int c = 0; c < 4; ++c) {
          const u32x2 z = h ? (u32x2){0u, 0u} : qb[c];
          f32x16 s = {}; s = mfma16(ka, qa[c], s); s = mfma8(kb, z, s);
#pragma unroll
          for (int i = 0; i < 16; ++i) mx[c] = fmaxf(mx[c], s[i]);
        }
      }
      pair_cnt += 2; lds_arrive(sy_pair); lds_wait(sy_pair, pair_cnt);
#pragma unroll
      for (int c = 0; c < 4; ++c) mx[c] = fmaxf(mx[c], xhalf(mx[c], h));
      *(LAS f32x4*)mine = (f32x4){mx[0], mx[1], mx[2], mx[3]};
      pair_cnt += 2; lds_arrive(sy_pair); lds_wait(sy_pair, pair_cnt);
      const f32x4 om = *(LAS f32x4*)theirs;
#pragma unroll
      for (int c = 0; c < 4; ++c) m[c] = fmaxf(mx[c], om[c ^ 2]);
      pair_cnt += 2; lds_arrive(sy_pair); lds_wait(sy_pair, pair_cnt);
    }
    {
      const unsigned ob = WS_OA + blk_id * 10240 + rg * 5120;
      u32x4 e;
#pragma unroll
      for (int t = 0; t < 2; ++t) {
        u32x4 a;
#pragma unroll
        for (int i = 0; i < 4; ++i) a[i] = pk(fin[t][2 * i] * linv[t], fin[t][2 * i + 1] * linv[t]);
        st128(rs, a, lq, ob + (2 * kg + t) * 1024);
        e[2 * t] = pk(fin[t][8] * linv[t], fin[t][9] * linv[t]); e[2 * t + 1] = pk(fin[t][10] * linv[t], fin[t][11] * linv[t]);
      }
      if (lq < 512u) st128(rs, e, lq, ob + 4096 + kg * 512);
    }
    b0 = (f32x16){}; b1 = (f32x16){}; b2 = (f32x16){};
  } else {
    const int hw = w - 4;
    const int job2 = rb * 4 + hw, pks2 = job2 % 40, ksl2 = pks2 % 5;
    const bool pack2 = extra && job2 < 80;
    unsigned p2s[5], p2d[5];
    {
      const unsigned ef = M_W + 2 * M_SLOT + hw * 6400 + (8 * h + q4) * (extra ? 40 : 320) + blk * 32 + p4 * 8;
      WAIT_VM(7);
      lds_arrive(L0 + M_SYNC + SY_KI);
      MAKE_TAG();
      WAIT_VM(0);
      f32x16 e = {};
      if (extra) {
        u32x4 a_[10], b_[10];
#pragma unroll
        for (int j = 0; j < 10; ++j) a_[j] = ldstr2(L + ef + j * 640, 160);
        lds_arrive(L0 + M_SYNC + SY_K4); lds_wait(L0 + M_SYNC + SY_K4, 4);
#pragma unroll
        for (int j = 0; j < 10; ++j) b_[j] = j < 8 ? lds128(L + ((M_X + mte * 8192 + hw * 16384 + xf) ^ (j << 5))) : lds128(L + ((M_X + mte * 8192 + 65536 + (xf ^ (hw << 6))) ^ ((j & 1) << 5)));
        SB();
        if (isV) {
#pragma unroll
          for (int j = 0; j < 10; ++j) e = mfma16(b_[j], a_[j], e);
        } else {
#pragma unroll
          for (int j = 0; j < 10; ++j) e = mfma16(a_[j], b_[j], e);
        }
        if (hw) {
#pragma unroll
          for (int q = 0; q < 4; ++q) *(LAS f32x4*)(L + M_KVP + (hw * 4 - 4 + q) * 1024 + l16) = (f32x4){e[4 * q], e[4 * q + 1], e[4 * q + 2], e[4 * q + 3]};
        }
      } else {
        u32x4 t[5];
#pragma unroll
        for (int j = 0; j < 5; ++j) t[j] = ldstr2(L + ef + j * 64, 1280);
#pragma unroll
        for (int j = 0; j < 5; ++j) {
          const int job = rb * 4 + hw, seg = job / 40, pks = job % 40;
#define PQ_AT(g10) (WS_PQ + (hg - 1 + ((g10) >= 5)) * 327680 + ((((seg * 5 + (g10) % 5) >> 2) * 40 + pks) * 2 + (((seg * 5 + (g10) % 5) >> 1) & 1)) * 1024 + ((seg * 5 + (g10) % 5) & 1) * 256)
          const unsigned u0 = rb < 30 ? PQ_AT(2 * j) : 0x7f000000u, u1 = rb < 30 ? PQ_AT(2 * j + 1) : 0x7f000000u;
          st128(rs, t[j], (blk ? u1 : u0) + (h * 32 + (lane & 15)) * 16, 0);
        }
      }
      if (hw) {
        lds_arrive(L0 + M_SYNC + SY_KP);
        if (!extra) { WAIT_VM(0); lds_arrive(L0 + M_SYNC + SY_F1); }
      } else {
        lds_wait(L0 + M_SYNC + SY_KP, 3);
        float ss = 0.f;
        if (extra) {
          f32x4 pv[12];
#pragma unroll
          for (int i = 0; i < 12; ++i) pv[i] = *(LAS f32x4*)(L + M_KVP + i * 1024 + l16);
          unsigned o[8];
#pragma unroll
          for (int q = 0; q < 4; ++q) {
            const f32x4 t = (pv[q] + pv[4 + q]) + (pv[8 + q] + (f32x4){e[4 * q], e[4 * q + 1], e[4 * q + 2], e[4 * q + 3]});
            o[2 * q] = pk(t[0], t[1]); o[2 * q + 1] = pk(t[2], t[3]);
          }
          const unsigned kv = WS_KV + (rb * 2 + mte) * TILE_B;
          if (isV) {
            const unsigned c1 = r == 20 ? 0x3f803f80u : 0u;
#pragma unroll
            for (int i = 0; i < 8; ++i) o[i] = r >= 20 ? c1 : o[i];
            st128(rs, (u32x4){o[0], o[1], o[2], o[3]}, l16, kv + 1536);
            st128(rs, (u32x4){o[4], o[5], o[6], o[7]}, l16, kv + 2560);
          } else {
            st128(rs, (u32x4){o[0], o[1], o[2], o[3]}, l16, kv);
            __builtin_amdgcn_raw_buffer_store_b64(h ? (u32x2){0x3f803f80u, 0x00003f80u} : (u32x2){o[4], o[5]}, rs, lane * 8, kv + 1024, AUX_SC1);
            ss = dot2(o[0], o[0], dot2(o[1], o[1], dot2(o[2], o[2], dot2(o[3], o[3], 0.f))));
            if (!h) ss = dot2(o[4], o[4], dot2(o[5], o[5], ss));
            ss = wave_max(ss + xhalf(ss, h));
          }
        }
        lds_arrive(L0 + M_SYNC + SY_KS);
        WAIT_VM(0);
        if (extra) { if (lane < 8) __hip_atomic_store(gran + lane * 128 + (isV ? 64 : 0) + rb * 2 + mte, ((unsigned long long)__float_as_uint(ss) << 32) | tag, RLX_AGENT); }
        else lds_wait(L0 + M_SYNC + SY_F1, 3);
        if (!extra && lane < 4) __hip_atomic_store((gu32*)(ws + WS_PF + ((hg >> 1) * 4 + lane) * 128 + rb * 4), tag, RLX_AGENT);
      }
      lds_wait(L0 + M_SYNC + SY_KI, 8);
      {
        const __attribute__((address_space(4))) u32x4* gq = (const __attribute__((address_space(4))) u32x4*)(gw + hw * 64);
        const unsigned xa = M_X + lane * 256 + ((((hw << 2) ^ lane) & 15) << 4);
        float s = 0.f, s2 = 0.f;
        u32x4 xv[20];
#pragma unroll
        for (int i = 0; i < 20; ++i) xv[i] = lds128(L + (xa ^ ((i & 3) << 4)) + (i >> 2) * 16384);
        SB();
#pragma unroll
        for (int i = 0; i < 20; ++i) { const u32x4 gv = gq[(i >> 2) * 16 + (i & 3)]; s = dot2(xv[i][0], gv[0], s); s2 = dot2(xv[i][1], gv[1], s2); s = dot2(xv[i][2], gv[2], s); s2 = dot2(xv[i][3], gv[3], s2); }
        *(LAS float*)(L + M_GATE + lane * 16 + hw * 4) = s + s2;
      }
      lds_arrive(L0 + M_SYNC + SY_G);
      bool pfok = false;
      if (hw == 1) {
        lds_wait(L0 + M_SYNC + SY_R2, 4);
        gu64* const gr = gran + (blockIdx.x & 7) * 128 + lane;
        unsigned long long gk = 0;
#pragma unroll 1
        for (unsigned spins = 0; spins < SPIN_CAP; ++spins) {
          gk = __hip_atomic_load(gr, RLX_AGENT);
          const unsigned long long gv = __hip_atomic_load(gr + 64, RLX_AGENT);
          pfok = __all(__hip_atomic_load((gu32*)(ws + WS_PF + ((hg >> 1) * 4 + (rb >> 3)) * 128 + r * 4), RLX_AGENT) == tag);
          if (__all((unsigned)gk == tag && (unsigned)gv == tag)) break;
          __builtin_amdgcn_s_sleep(2);
        }
        __builtin_amdgcn_fence(__ATOMIC_ACQUIRE, "wavefront");
        if (lane < 56) touch_line_sc1(ws + WS_KV + (bidx * 56 + lane) * 128, L0 + M_DUMP);
        const float t = wave_max(__uint_as_float((unsigned)(gk >> 32)));
        if (lane == 0) *(LAS float*)(L + M_SYNC + SY_KK) = t;
        lds_arrive(L0 + M_SYNC + SY_KV);
        if (pfok) lds_arrive(L0 + M_SYNC + SY_PR);
      }
#pragma unroll
      for (int i = 0; i < 5; ++i) {
        p2s[i] = pack_off(i, lane, 1280, ksl2 < 4 ? 4 : 40, ksl2 < 4 ? 8 : 20);
        const int g10 = 2 * i + blk, hh = g10 >= 5, G = g10 - 5 * hh;
        p2d[i] = (job2 < 40 ? WS_PA : WS_PB) + pks2 * 2560 + (hg + hh) * 102400 + (G >> 1) * 1024 + (G < 4 ? h * 32 + (G & 1) * 16 + (lane & 15) : h * 16 + (lane & 15)) * 16;
        asm volatile("" : "+v"(p2s[i]), "+v"(p2d[i]));
      }
      if (hw == 1 && !pfok) { lds_wait(L0 + M_SYNC + SY_GO, 4); glb_wait((gu32*)(ws + WS_PF + ((hg >> 1) * 4 + (rb >> 3)) * 128 + r * 4), tag); lds_arrive(L0 + M_SYNC + SY_PR); }
      lds_wait(L0 + M_SYNC + SY_PR, 1);
      lds_wait(L0 + M_SYNC + SY_GO, 4);
    }
    const unsigned l16c = blk ? 0x7fffffffu : (h * 16 + (lane & 15)) * 16;
    f32x16 a00, a01, a10, a11;
    {
      constexpr int WD = 4;
      u32x4 w0[WD], w1[WD];
      const unsigned pq = WS_PQ + (hg * 4 + hw) * 81920, l16h = hw == 3 && blk ? 0x7fffffffu : l16;
#pragma unroll
      for (int i = 0; i < WD; ++i) { w0[i] = BLD128(l16, pq + i * 2048, AUX_SC1); w1[i] = BLD128(l16h, pq + i * 2048 + 1024, AUX_SC1); }
      __builtin_amdgcn_s_setprio(1);
      u32x4 x0 = lds128(L + M_X + xf), x1 = lds128(L + M_X + 8192 + xf);
#pragma unroll
      for (int ks = 0; ks < 40; ++ks) {
        const int sl = ks % WD, kn = ks < 39 ? ks + 1 : ks;
        const u32x4 n0 = lds128(L + M_X + (kn >> 3) * 16384 + (xf ^ ((kn & 7) << 5))), n1 = lds128(L + M_X + (kn >> 3) * 16384 + 8192 + (xf ^ ((kn & 7) << 5)));
        SB();
        if (ks == 0) { const f32x16 z = {}; a00 = mfma16(w0[sl], x0, z); a01 = mfma16(w1[sl], x0, z); a10 = mfma16(w0[sl], x1, z); a11 = mfma16(w1[sl], x1, z); }
        else { a00 = mfma16(w0[sl], x0, a00); a01 = mfma16(w1[sl], x0, a01); a10 = mfma16(w0[sl], x1, a10); a11 = mfma16(w1[sl], x1, a11); }
        SB();
        if (ks + WD < 40) { w0[sl] = BLD128(l16, pq + (ks + WD) * 2048, AUX_SC1); w1[sl] = BLD128(l16h, pq + (ks + WD) * 2048 + 1024, AUX_SC1); }
        x0 = n0; x1 = n1;
        if (ks % 8 == 0 && ks < 32 && lane < 16) touch_line_sc1(ws + pq + (ks + 8) * 2048 + (rb & 7) * 2048 + lane * 128, L0 + M_DUMP);
        if (ks == 6) {
#pragma unroll
          for (int i = 0; i < 3; ++i) st128(rs, (u32x4){~0u, ~0u, ~0u, ~0u}, hw * 3 + i < 10 ? l16 : 0x7fffffffu, WS_OA + blk_id * 10240 + (hw * 3 + i) * 1024);
          if (pack2) {
            lds_wait(L0 + M_SYNC + SY_Q, 4);
            const char* src = (job2 < 40 ? woA : woB) + (pks2 / 5) * 102400 + (ksl2 < 4 ? 20 * ksl2 : 16) * 1280 + hg * 160;
#pragma unroll
            for (int i = 0; i < 5; ++i) glds16(src, p2s[i], L0 + M_W + hw * 6400 + i * 1024);
          }
        }
        if (ks == 12 && extra) {
          if (pack2) {
            WAIT_VM(8);
            const lptr st = L + M_W + hw * 6400 + (8 * h + q4) * 320 + blk * 32 + p4 * 8;
#pragma unroll
            for (int j = 0; j < 5; ++j) st128(rs, ldstr2(st + j * 64, 1280), p2d[j], 0);
          }
        }
        if (ks == 18 && extra) {
          WAIT_VM(8);
          if (lds_arrive_last(L0 + M_SYNC + SY_PK, 3) && lane < 4) __hip_atomic_store((gu32*)(ws + WS_PG + ((hg >> 1) * 4 + lane) * 128 + rb * 4), tag, RLX_AGENT);
        }
      }
      __builtin_amdgcn_s_setprio(2);
    }
    {
      const unsigned hb = WS_HALO + blk_id * 5120;
      lds_wait(L0 + M_SYNC + SY_Q, 4); lds_wait(L0 + M_SYNC + SY_G, 4);
      const lptr ip = L + M_IMG + (16 + r) * IMG_P + hw * 128 + h * 8;
      const unsigned hv = r >= 16 ? (r - 16) * 320 + h * 8 : 0x7fffffffu;
#define QKV_OUT(A, MT, NT) _Pragma("unroll") for (int gq = 0; gq < 4; ++gq) { const int N = 64 * hw + 32 * (NT) + 8 * gq; \
        if (((NT) == 0 || gq < 2) || hw < 3) { const u32x2 v = {pk(A[4 * gq], A[4 * gq + 1]), pk(A[4 * gq + 2], A[4 * gq + 3])}; \
          *(LAS u32x2*)(ip + (MT) * 32 * IMG_P + (NT) * 64 + gq * 16) = v; \
          if ((MT) == 1 && N >= 80) __builtin_amdgcn_raw_buffer_store_b64(v, rs, hv, hb + (N - 80) * 2, AUX_SC1); } }
      QKV_OUT(a10, 1, 0) QKV_OUT(a11, 1, 1) QKV_OUT(a00, 0, 0) QKV_OUT(a01, 0, 1)
      WAIT_VM(0);
      if (lds_arrive_last(L0 + M_SYNC + SY_HP, 3) && lane == 0) __hip_atomic_store((gu32*)(ws + WS_HF + blk_id * 128), tag, RLX_AGENT);
      glb_wait((gu32*)(ws + WS_PG + ((hg >> 1) * 4 + (rb >> 3)) * 128 + r * 4), tag);
      {
        u32x4 wa[13];
#pragma unroll
        for (int i = 0; i < 13; ++i) wa[i] = BLD128(l16, WS_PA + hg * 102400 + hw * 25600 + i * 1024, AUX_SC1);
        lds_wait_slow(L0 + M_SYNC + SY_HP, 4);
#pragma unroll
        for (int i = 0; i < 13; ++i) *(LAS u32x4*)(L + M_WO + hw * 25600 + i * 1024 + l16) = wa[i];
        SB();
#pragma unroll
        for (int i = 0; i < 12; ++i) wa[i] = BLD128(l16, WS_PA + hg * 102400 + hw * 25600 + (13 + i) * 1024, AUX_SC1);
        if (hw == 2) { if (rb > 0) glb_wait((gu32*)(ws + WS_HF + (blk_id - 8) * 128), tag); lds_arrive(L0 + M_SYNC + SY_HR); }
#pragma unroll
        for (int i = 0; i < 12; ++i) *(LAS u32x4*)(L + M_WO + hw * 25600 + (13 + i) * 1024 + l16) = wa[i];
      }
      lds_wait_slow(L0 + M_SYNC + SY_HR, 1);
      const int row = lane >> 2, off = (lane & 2) * 80 + hw * 40 + (lane & 1) * 24;
      const unsigned src = rb > 0 ? row * 320 + off : 0x7fffffffu;
      const u32x2 v0 = __builtin_amdgcn_raw_buffer_load_b64(rs, src, hb - 8 * 5120, AUX_SC1), v1 = __builtin_amdgcn_raw_buffer_load_b64(rs, src, hb - 8 * 5120 + 8, AUX_SC1);
      const u32x2 v2 = __builtin_amdgcn_raw_buffer_load_b64(rs, src - (lane & 1) * 8, hb - 8 * 5120 + 16, AUX_SC1);
      const lptr dp = L + M_IMG + row * IMG_P + 160 + off;
      *(LAS u32x2*)dp = v0; *(LAS u32x2*)(dp + 8) = v1; *(LAS u32x2*)(dp + 16 - (lane & 1) * 8) = v2;
    }
    u32x4 wf[30];
#define WF_LOAD(j0, j1) _Pragma("unroll") for (int j = j0; j < j1; ++j) wf[j] = BLD128(j % 3 == 2 ? l16c : l16, WS_PB + hg * 102400 + hw * 25600 + (j / 3) * 2560 + (j % 3) * 1024, AUX_SC1)
    WF_LOAD(0, 12);
    {
      const lptr p = L + M_IMG + (16 + lane) * IMG_P + hw * 40;
      const float g1 = 1.f - gate_of(*(LAS f32x4*)(L + M_GATE + lane * 16));
      u32x2 q[5], kv[8][5];
#pragma unroll
      for (int i = 0; i < 5; ++i) q[i] = lds64(p + i * 8);
#pragma unroll
      for (int j = 0; j < 8; ++j)
#pragma unroll
        for (int i = 0; i < 5; ++i) kv[j][i] = lds64(p + 160 - 2 * j * IMG_P + i * 8);
      SB();
      float lg[8];
#pragma unroll
      for (int j = 0; j < 8; ++j) {
        float a = dot2(q[0][0], kv[j][0][0], 0.f);
        a = dot2(q[0][1], kv[j][0][1], a);
#pragma unroll
        for (int i = 1; i < 5; ++i) { a = dot2(q[i][0], kv[j][i][0], a); a = dot2(q[i][1], kv[j][i][1], a); }
        lg[j] = a;
      }
      SB();
#pragma unroll
      for (int j = 0; j < 8; ++j)
#pragma unroll
        for (int i = 0; i < 5; ++i) kv[j][i] = lds64(p + 320 - 2 * j * IMG_P + i * 8);
      WF_LOAD(12, 21);
      if (rb == 0) {
#pragma unroll
        for (int j = 1; j < 8; ++j) lg[j] = lane >= 2 * j ? lg[j] : -1e9f / 0.22360679774997896f;
      }
      const float mx = -QSCALE * fmaxf(fmaxf(fmaxf(lg[0], lg[1]), fmaxf(lg[2], lg[3])), fmaxf(fmaxf(lg[4], lg[5]), fmaxf(lg[6], lg[7])));
#pragma unroll
      for (int j = 0; j < 8; ++j) lg[j] = __builtin_amdgcn_exp2f(__builtin_fmaf(lg[j], QSCALE, mx));
      const float inv = __builtin_amdgcn_rcpf(((lg[0] + lg[1]) + (lg[2] + lg[3])) + ((lg[4] + lg[5]) + (lg[6] + lg[7])));
      float o[20];
#pragma unroll
      for (int j = 0; j < 8; j += 2) {
        const unsigned pp = pk(lg[j] * inv, lg[j + 1] * inv), pl0 = pp & 0xffffu, ph0 = pp << 16, pl1 = pp >> 16, ph1 = pp & 0xffff0000u;
#pragma unroll
        for (int i = 0; i < 5; ++i) {
          const u32x2 v = kv[j][i], u = kv[j + 1][i];
          if (j == 0) { o[4 * i] = dot2(v[0], pl0, 0.f); o[4 * i + 1] = dot2(v[0], ph0, 0.f); o[4 * i + 2] = dot2(v[1], pl0, 0.f); o[4 * i + 3] = dot2(v[1], ph0, 0.f); }
          else { o[4 * i] = dot2(v[0], pl0, o[4 * i]); o[4 * i + 1] = dot2(v[0], ph0, o[4 * i + 1]); o[4 * i + 2] = dot2(v[1], pl0, o[4 * i + 2]); o[4 * i + 3] = dot2(v[1], ph0, o[4 * i + 3]); }
          o[4 * i] = dot2(u[0], pl1, o[4 * i]); o[4 * i + 1] = dot2(u[0], ph1, o[4 * i + 1]); o[4 * i + 2] = dot2(u[1], pl1, o[4 * i + 2]); o[4 * i + 3] = dot2(u[1], ph1, o[4 * i + 3]);
        }
      }
      unsigned u[10];
#pragma unroll
      for (int i = 0; i < 10; ++i) u[i] = pk(o[2 * i] * g1, o[2 * i + 1] * g1);
      u32x4 fa, fb;
#pragma unroll
      for (int i = 0; i < 4; ++i) { const int a = (i >> 1) * 4 + (i & 1); auto sw = __builtin_amdgcn_permlane32_swap(u[a], u[a + 2], false, false); fa[i] = sw[0]; fb[i] = sw[1]; }
      const unsigned ob = WS_OB + blk_id * 10240;
      st128(rs, fa, l16, ob + hw * 1024);
      st128(rs, fb, l16, ob + 5120 + hw * 1024);
      __builtin_amdgcn_raw_buffer_store_b64((u32x2){u[8], u[9]}, rs, h * 5120 + r * 16, ob + 4096 + (hw >> 1) * 512 + (hw & 1) * 8, AUX_SC1);
      WAIT_VM(0);
      if (lds_arrive_last(L0 + M_SYNC + SY_OB, 3) && lane == 0) __hip_atomic_store((gu32*)(ws + WS_RF + rb * 128 + hg * 4), tag, RLX_AGENT);
      WF_LOAD(21, 30);
    }
    {
      if (blockIdx.x == 0 && hw == 0) {
#pragma unroll 1
        for (unsigned spins = 0; spins < SPIN_CAP; ++spins) {
          bool ok = true;
#pragma unroll
          for (int i = 0; i < 2; ++i) ok &= (unsigned)__hip_atomic_load(gran + i * 64 + lane, RLX_AGENT) == tag && __hip_atomic_load((gu32*)(ws + WS_PF + (i * 2 + h) * 512 + r * 4), RLX_AGENT) == tag;
          if (__all(ok)) break;
        }
        if (lane == 0) __hip_atomic_store((gu32*)(ws + WS_EPOCH), tag, RLX_AGENT);
      }
      u32x4 of[10];
#define OB_LOAD(mt_) _Pragma("unroll") for (int j = 0; j < 10; ++j) { of[j] = BLD128(l16, WS_OB + (rb * 8 + hw * 2 + j / 5) * 10240 + (mt_) * 5120 + (j % 5) * 1024, AUX_SC1); }
      if (hw == 3) { glb_wait((gu32*)(ws + WS_RF + rb * 128 + (lane & 7) * 4), tag); lds_arrive(L0 + M_SYNC + SY_BR); }
      lds_wait_slow(L0 + M_SYNC + SY_BR, 1);
      OB_LOAD((hw & 1) ^ 1);
      __builtin_amdgcn_s_setprio(0);
#define OB_MMA(first) _Pragma("unroll") for (int j = 0; j < 10; ++j) { const f32x16 z = {}; \
        b0 = mfma16(wf[3 * j], of[j], first && j == 0 ? z : b0); b1 = mfma16(wf[3 * j + 1], of[j], first && j == 0 ? z : b1); b2 = mfma16(wf[3 * j + 2], of[j], first && j == 0 ? z : b2); }
      OB_MMA(true);
      __builtin_amdgcn_s_setprio(2);
      OB_LOAD(hw & 1);
      lds_wait_slow(L0 + M_SYNC + SY_OB, 4);
      {
        f32x4 qs[10];
        QUADS(qs, b0, b1, b2);
#pragma unroll
        for (int q = 0; q < 10; ++q) *(LAS f32x4*)(L + M_RED + hw * 10240 + q * 1024 + l16) = qs[q];
      }
      lds_arrive(L0 + M_SYNC + SY_B); lds_wait_slow(L0 + M_SYNC + SY_B, 4);
#pragma unroll
      for (int q = 0; q < 10; ++q) {
        const f32x4 v = *(LAS f32x4*)(L + M_RED + (hw ^ 1) * 10240 + q * 1024 + l16);
#pragma unroll
        for (int j = 0; j < 4; ++j) { if (q < 4) b0[4 * q + j] = v[j]; else if (q < 8) b1[4 * (q - 4) + j] = v[j]; else b2[4 * (q - 8) + j] = v[j]; }
      }
      lds_arrive(L0 + M_SYNC + SY_HD);
      __builtin_amdgcn_s_setprio(0);
      lds_wait_mid(L0 + M_SYNC + SY_AD, 1);
      OB_MMA(false);
    }
    __builtin_amdgcn_s_setprio(0);
  }
  {
    unsigned z_ = 0u; asm volatile("" : "+v"(z_));
    const int ln = __builtin_amdgcn_mbcnt_hi(~0u, __builtin_amdgcn_mbcnt_lo(~0u, z_));
    const int mt = w & 1, kq = w >> 1, hh = ln >> 5;
    const unsigned l16 = ln * 16, l16c = (hh * 16 + (ln & 15)) * 16;
    lds_wait(L0 + M_SYNC + SY_HD, 4);
    u32x4 wfr[30];
#pragma unroll
    for (int j = 0; j < 30; ++j) wfr[j] = lds128(L + M_WO + kq * 25600 + (j / 3) * 2560 + (j % 3) * 1024 + (j % 3 == 2 ? l16c : l16));
    BAR();
    u32x4 of[10];
#define OA_OFF(j) (WS_OA + (rb * 8 + kq * 2 + (j) / 5) * 10240 + mt * 5120 + ((j) % 5) * 1024)
#define OA_LOAD(g) _Pragma("unroll") for (int j = 5 * (g); j < 5 * (g) + 5; ++j) of[j] = BLD128(l16, OA_OFF(j), AUX_SC1)
#define OA_M4(j) umax(umax(of[j][0], of[j][1]), umax(of[j][2], of[j][3]))
#define OA_OK(g) __all(umax(umax(umax(OA_M4(5 * (g)), OA_M4(5 * (g) + 1)), umax(OA_M4(5 * (g) + 2), OA_M4(5 * (g) + 3))), OA_M4(5 * (g) + 4)) != ~0u)
#define OA_RETRY(need) _Pragma("unroll 1") for (unsigned spins = 0; spins < DATA_CAP && !(need); ++spins) { \
      const unsigned long long miss = __ballot(__builtin_amdgcn_raw_buffer_load_b32(rs, pw, OA_OFF(0), AUX_SC1) == ~0u); \
      const bool r0 = !ok0 && !(miss & 0x3ffu), r1 = !ok1 && !(miss & 0xffc00u); \
      if (r0) { OA_LOAD(0); } if (r1) { OA_LOAD(1); } \
      if (r0) ok0 = OA_OK(0); if (r1) ok1 = OA_OK(1); \
      if (!r0 && !r1) __builtin_amdgcn_s_sleep(1); }
    const unsigned pw = ln < 10 ? ln * 512 : ln < 20 ? 10240 + (ln - 10) * 512 : 0x7fffffffu;
    OA_LOAD(0); OA_LOAD(1);
    bool ok0 = OA_OK(0), ok1 = OA_OK(1);
    OA_RETRY(ok0);
#pragma unroll
    for (int j = 0; j < 5; ++j) { b0 = mfma16(wfr[j * 3], of[j], b0); b1 = mfma16(wfr[j * 3 + 1], of[j], b1); b2 = mfma16(wfr[j * 3 + 2], of[j], b2); }
    OA_RETRY(ok1);
#pragma unroll
    for (int j = 5; j < 10; ++j) { b0 = mfma16(wfr[j * 3], of[j], b0); b1 = mfma16(wfr[j * 3 + 1], of[j], b1); b2 = mfma16(wfr[j * 3 + 2], of[j], b2); }
    asm volatile("" : "+v"(b0), "+v"(b1), "+v"(b2));
    f32x4 qd[10];
    QUADS(qd, b0, b1, b2);
#pragma unroll
    for (int q = 0; q < 10; ++q) *(LAS f32x4*)(L + (w * 10 + q) * 1040 + ((ln & 24) * 2 + ((ln * 10) & 15) + hh) * 16) = qd[q];
    BAR();
    {
      const __amdgpu_buffer_rsrc_t ro = __builtin_amdgcn_make_buffer_rsrc((void*)out, 0, 2621440, 0x00020000);
      f32x4 v[2][8];
      unsigned oo[2];
#pragma unroll
      for (int i = 0; i < 2; ++i) {
        const unsigned P = i * 64 + (i ? ln & 15 : ln), ri = (P * 6554u) >> 16, q = P - ri * 10, row = w * 8 + ri;
        const lptr p = L + ((row >> 5) * 10 + q) * 1040 + ((row & 24) * 2 + ((row * 10) & 15)) * 16;
#pragma unroll
        for (int j = 0; j < 8; ++j) v[i][j] = *(LAS f32x4*)(p + (j >> 1) * 20800 + (j & 1) * 16);
        oo[i] = i == 0 || ln < 16 ? (rb * 64 + row) * 1280 + hg * 160 + q * 16 : 0x7fffffffu;
      }
#pragma unroll
      for (int i = 0; i < 2; ++i) {
        const f32x4 a = (v[i][0] + v[i][2]) + (v[i][4] + v[i][6]), b = (v[i][1] + v[i][3]) + (v[i][5] + v[i][7]);
        __builtin_amdgcn_raw_buffer_store_b128((u32x4){pk(a[0], a[1]), pk(a[2], a[3]), pk(b[0], b[1]), pk(b[2], b[3])}, ro, oo[i], 0, AUX_SC1);
      }
    }
  }
  asm volatile("k_main_end:");
}

extern "C" void kernel_launch(void* const* d_in, const int* in_sizes, int n_in, void* d_out, int out_size, void* d_ws, size_t ws_size, hipStream_t stream) {
  (void)in_sizes; (void)n_in; (void)out_size; (void)ws_size;
  (void)hipFuncSetAttribute((const void*)k_main, hipFuncAttributeMaxDynamicSharedMemorySize, M_LDS);
  k_main<<<256, 512, M_LDS, stream>>>((const char*)d_in[0], (const char*)d_in[1], (const char*)d_in[2], (const char*)d_in[3], (const char*)d_in[4],
                                      (const char*)d_in[6], (const char*)d_in[7], (const char*)d_in[8], (char*)d_ws, (char*)d_out);
}
```
